```python
import torch

def _softmax(x, axis=-1):
    return torch.softmax(x, dim=axis)
def _gelu(x):
    return torch.nn.functional.gelu(x, approximate="tanh")
def _silu(x):
    return torch.nn.functional.silu(x)
def _sigmoid(x):
    return torch.sigmoid(x)
def _layernorm(x, axis=-1, eps=1e-5):
    m = x.mean(dim=axis, keepdim=True)
    v = x.var(dim=axis, keepdim=True, unbiased=False)
    return (x - m) / torch.sqrt(v + eps)
def _scatter_add(tgt, idx, src, axis=0):
    return tgt.clone().index_add_(axis, idx, src)
def _segment_sum(src, seg, n):
    out = torch.zeros((n,) + src.shape[1:], dtype=src.dtype)
    return out.index_add_(0, seg, src)
def _pad(x, pw):
    flat = tuple(v for pair in reversed(pw) for v in pair)
    return torch.nn.functional.pad(x, flat)

kernel_name = 'standalone__mixer_s_mlp__prompt'

def setup_inputs():

    _w = (lambda a: (lambda t: t.to(torch.bfloat16) if t.is_floating_point() else t)(torch.as_tensor(a)))
    B, S, D, F = 1, 2048, 512, 1792
    return {
        "x":  _w((lambda *s: torch.randn(*s, dtype=torch.bfloat16) * 0.4)(B, S, D)),
        "w1": _w((lambda *s: torch.randn(*s, dtype=torch.bfloat16) * 3.2)(D, D) * (D ** -0.5)),
        "w2": _w((lambda *s: torch.randn(*s, dtype=torch.bfloat16) * 3.2)(D, D) * (D ** -0.5)),
        "w3": _w((lambda *s: torch.randn(*s, dtype=torch.bfloat16) * 3.2)(D, F) * (D ** -0.5)),
        "w4": _w((lambda *s: torch.randn(*s, dtype=torch.bfloat16) * 0.8)(F, D) * (F ** -0.5)),
    }

def reference(x, w1, w2, w3, w4):
    B, S, D = x.shape
    h = _gelu(_layernorm(x, axis=-1) @ w1)
    c = D // 4
    g0 = _pad(h[:, :, 0*c:1*c], ((0, 0), (1, 0), (0, 0)))[:, :S, :]
    g1 = _pad(h[:, :, 1*c:2*c], ((0, 0), (0, 1), (0, 0)))[:, 1:, :]
    g2 = _pad(h[:, :, 2*c:3*c], ((0, 0), (2, 0), (0, 0)))[:, :S, :]
    g3 = _pad(h[:, :, 3*c:   ], ((0, 0), (0, 2), (0, 0)))[:, 2:, :]
    hs = torch.concatenate([g0, g1, g2, g3], axis=-1) @ w2
    x1 = x + hs
    h2 = _gelu(_layernorm(x1, axis=-1) @ w3) @ w4
    return x1 + h2
```

```cpp
#include <hip/hip_runtime.h>
#include <hip/hip_bf16.h>
typedef short bf16x8 __attribute__((ext_vector_type(8)));
typedef float f32x4 __attribute__((ext_vector_type(4)));
typedef unsigned short u16;
constexpr int S=2048, D=512, F=1792;
__device__ __forceinline__ float bf2f(u16 b){ return __builtin_bit_cast(float,(unsigned)b<<16);}
__device__ __forceinline__ u16 f2bf(float f){ __hip_bfloat16 h=__float2bfloat16(f); return __builtin_bit_cast(u16,h);}
__device__ __forceinline__ float gelu(float v){ float a=v*(-2.302208198f-0.1029432397f*v*v); return v/(1.f+exp2f(a)); }
template<int MODE,int K,int N>
__global__ __launch_bounds__(256) void gemm(const u16* __restrict__ A,const u16* __restrict__ W,const u16* __restrict__ R,u16* __restrict__ O){
  __shared__ u16 As[64][40];
  __shared__ u16 Bs[64][40];
  __shared__ float mean[64], rstd[64];
  int bn=blockIdx.x, bm=blockIdx.y; int tid=threadIdx.x, lane=tid&63, w=tid>>6;
  int r0=bm*64, n0=bn*64; int sh=0;
  if(MODE==0){ int g=n0/128; sh = g==0?-1: g==1?1: g==2?-2:2; }
  if(MODE==0||MODE==2){
    for(int r=w;r<64;r+=4){ int row=r0+r+sh; float s=0,s2=0; float v[8];
      for(int j=0;j<8;j++){ v[j]= (row>=0&&row<S)? bf2f(A[(size_t)row*K+lane*8+j]):0.f; s+=v[j]; }
      for(int o=32;o;o>>=1) s+=__shfl_xor(s,o); float m=s/K;
      for(int j=0;j<8;j++){ float d=v[j]-m; s2+=d*d; }
      for(int o=32;o;o>>=1) s2+=__shfl_xor(s2,o);
      if(lane==0){mean[r]=m; rstd[r]=rsqrtf(s2/K+1e-5f);} }
    __syncthreads();
  }
  f32x4 acc[4]={};
  for(int k0=0;k0<K;k0+=32){
    { int r=tid>>2, c=(tid&3)*8; int row=r0+r+sh;
      for(int j=0;j<8;j++){ float v=(row>=0&&row<S)? bf2f(A[(size_t)row*K+k0+c+j]):0.f; if(MODE==0||MODE==2) v=(v-mean[r])*rstd[r]; As[r][c+j]=f2bf(v);} }
    { int k=tid>>3, c=(tid&7)*8; for(int j=0;j<8;j++) Bs[c+j][k]=W[(size_t)(k0+k)*N+n0+c+j]; }
    __syncthreads();
    bf16x8 a=*(bf16x8*)&As[w*16+(lane&15)][(lane>>4)*8];
    for(int n=0;n<4;n++){ bf16x8 b=*(bf16x8*)&Bs[n*16+(lane&15)][(lane>>4)*8]; acc[n]=__builtin_amdgcn_mfma_f32_16x16x32_bf16(a,b,acc[n],0,0,0);}
    __syncthreads();
  }
  for(int n=0;n<4;n++) for(int j=0;j<4;j++){ int row=r0+w*16+(lane>>4)*4+j, col=n0+n*16+(lane&15); float v=acc[n][j];
    if(MODE==0||MODE==2) v=gelu(v); else v+=bf2f(R[(size_t)row*N+col]);
    O[(size_t)row*N+col]=f2bf(v);}
}
extern "C" void kernel_launch(void* const* d_in,const int* in_sizes,int n_in,void* d_out,int out_size,void* d_ws,size_t ws_size,hipStream_t stream){
  const u16* x=(const u16*)d_in[0]; const u16* w1=(const u16*)d_in[1]; const u16* w2=(const u16*)d_in[2]; const u16* w3=(const u16*)d_in[3]; const u16* w4=(const u16*)d_in[4];
  u16* h=(u16*)d_ws; u16* x1=h+S*D; u16* g=x1+S*D;
  gemm<0,D,D><<<dim3(8,32),256,0,stream>>>(x,w1,nullptr,h);
  gemm<1,D,D><<<dim3(8,32),256,0,stream>>>(h,w2,x,x1);
  gemm<2,D,F><<<dim3(28,32),256,0,stream>>>(x1,w3,nullptr,g);
  gemm<3,F,D><<<dim3(8,32),256,0,stream>>>(g,w4,x1,(u16*)d_out);
}
```

```cpp
#include <hip/hip_runtime.h>
#define LAS __attribute__((address_space(3)))
typedef __bf16 bf16x8 __attribute__((ext_vector_type(8)));
typedef __bf16 bf16x2 __attribute__((ext_vector_type(2)));
typedef short s16x4 __attribute__((ext_vector_type(4)));
typedef short s16x8 __attribute__((ext_vector_type(8)));
typedef float f32x4 __attribute__((ext_vector_type(4)));
typedef float f32x2 __attribute__((ext_vector_type(2)));
typedef unsigned v4u __attribute__((ext_vector_type(4)));
typedef unsigned v2u __attribute__((ext_vector_type(2)));
typedef __amdgpu_buffer_rsrc_t srd_t;

constexpr unsigned K1 = 1024u;
constexpr unsigned WS_EP = 0, WS_FL = 8192, WS_H = 65536, WS_X1 = WS_H + 32u * 65536u, WS_G = WS_X1 + 32u * 65536u,
                   WS_END = WS_G + 32u * 229376u;
constexpr unsigned L_X1 = 96 * K1, L_EX = 80 * K1, L_ST = 92 * K1, L_FLG = 93 * K1, L_TOTAL = 160 * K1;
constexpr unsigned OOB = 0x80000000u;
constexpr unsigned SPIN_CAP = 200000u;

#define FI __device__ __forceinline__
FI void dma(srd_t s, unsigned voff, unsigned soff, unsigned lds) {
  asm volatile("s_mov_b32 m0, %0\n\ts_nop 0\n\tbuffer_load_dwordx4 %1, %2, %3 offen lds" ::"s"(lds), "v"(voff), "s"(s), "s"(soff) : "memory");
}
FI void dma_sc1(srd_t s, unsigned voff, unsigned soff, unsigned lds) {
  asm volatile("s_mov_b32 m0, %0\n\ts_nop 0\n\tbuffer_load_dwordx4 %1, %2, %3 offen sc1 lds" ::"s"(lds), "v"(voff), "s"(s), "s"(soff) : "memory");
}
template <int OFF> FI void ld4_sc1(v4u& d, srd_t s, unsigned voff, unsigned soff) {
  asm volatile("buffer_load_dwordx4 %0, %1, %2, %3 offen offset:%4 sc1" : "=v"(d) : "v"(voff), "s"(s), "s"(soff), "n"(OFF) : "memory");
}
FI void ld4(v4u& d, srd_t s, unsigned voff, unsigned soff) {
  asm volatile("buffer_load_dwordx4 %0, %1, %2, %3 offen" : "=v"(d) : "v"(voff), "s"(s), "s"(soff) : "memory");
}
FI void ld2_sc1(v2u& d, srd_t s, unsigned voff, unsigned soff) {
  asm volatile("buffer_load_dwordx2 %0, %1, %2, %3 offen sc1" : "=v"(d) : "v"(voff), "s"(s), "s"(soff) : "memory");
}
FI void st4_sc1(v4u d, srd_t s, unsigned voff, unsigned soff) {
  asm volatile("buffer_store_dwordx4 %0, %1, %2, %3 offen sc1\n\ts_nop 1" ::"v"(d), "v"(voff), "s"(s), "s"(soff) : "memory");
}
FI void st4(v4u d, srd_t s, unsigned voff, unsigned soff) {
  asm volatile("buffer_store_dwordx4 %0, %1, %2, %3 offen\n\ts_nop 1" ::"v"(d), "v"(voff), "s"(s), "s"(soff) : "memory");
}
FI void st2_sc1(v2u d, srd_t s, unsigned voff, unsigned soff) {
  asm volatile("buffer_store_dwordx2 %0, %1, %2, %3 offen sc1" ::"v"(d), "v"(voff), "s"(s), "s"(soff) : "memory");
}
template <int N> FI void vwait() { asm volatile("s_waitcnt vmcnt(%0)" ::"n"(N) : "memory"); }
FI void bar() { asm volatile("s_waitcnt lgkmcnt(0)\n\ts_barrier" ::: "memory"); }
template <int N> FI void vbar() { asm volatile("s_waitcnt vmcnt(%0) lgkmcnt(0)\n\ts_barrier" ::"n"(N) : "memory"); }
FI void tie(v4u& a) { asm volatile("" : "+v"(a)); }
FI void tie2(v2u& a) { asm volatile("" : "+v"(a)); }

FI bf16x8 trfrag(unsigned a, unsigned second) {
  s16x4 lo = __builtin_amdgcn_ds_read_tr16_b64_v4i16((LAS s16x4*)a);
  s16x4 hi = __builtin_amdgcn_ds_read_tr16_b64_v4i16((LAS s16x4*)(a + second));
  s16x8 r = __builtin_shufflevector(lo, hi, 0, 1, 2, 3, 4, 5, 6, 7);
  return __builtin_bit_cast(bf16x8, r);
}
FI v4u lds128(unsigned a) { return *(LAS v4u*)a; }
FI f32x4 mfma(bf16x8 a, bf16x8 b, f32x4 c) { return __builtin_amdgcn_mfma_f32_16x16x32_bf16(a, b, c, 0, 0, 0); }
FI bf16x8 asb(v4u v) { return __builtin_bit_cast(bf16x8, v); }
FI unsigned pk(float a, float b) {
  f32x2 f = {a, b};
  return __builtin_bit_cast(unsigned, __builtin_convertvector(f, bf16x2));
}
FI unsigned umin(unsigned a, unsigned b) { return a < b ? a : b; }
FI unsigned umax(unsigned a, unsigned b) { return a > b ? a : b; }
FI unsigned max4(unsigned m, v4u v) { return umax(umax(umax(m, v.x), v.y), umax(v.z, v.w)); }
FI float blo(unsigned u) { return __builtin_bit_cast(float, u << 16); }
FI float bhi(unsigned u) { return __builtin_bit_cast(float, u & 0xffff0000u); }
FI float gelu_n(float a, float k1, float k2, float irs) {
  float u = __builtin_fmaf(a * a, k2, k1);
  float e = __builtin_amdgcn_exp2f(a * u);
  return a * __builtin_amdgcn_rcpf(__builtin_fmaf(e, irs, irs));
}
FI v4u gelu_frag(f32x4 a0, f32x4 a1, f32x4 c0, f32x4 c1, f32x4 st) {
  float o[8];
#pragma unroll
  for (int j = 0; j < 4; j++) {
    o[j] = gelu_n(__builtin_fmaf(-st.x, c0[j], a0[j]), st.y, st.z, st.w);
    o[4 + j] = gelu_n(__builtin_fmaf(-st.x, c1[j], a1[j]), st.y, st.z, st.w);
  }
  v4u r = {pk(o[0], o[1]), pk(o[2], o[3]), pk(o[4], o[5]), pk(o[6], o[7])};
  r.x = umin(r.x, 0xFFFFFFFEu); r.y = umin(r.y, 0xFFFFFFFEu); r.z = umin(r.z, 0xFFFFFFFEu); r.w = umin(r.w, 0xFFFFFFFEu);
  return r;
}
FI f32x4 row_stats(f32x4 rs, f32x4 sq, int lane) {
  int t = lane & 15;
  float d = (t & 2) ? ((t & 1) ? sq.w : sq.z) : ((t & 1) ? sq.y : sq.x);
  float s2 = __builtin_bit_cast(float, __builtin_amdgcn_ds_bpermute((20 * (t >> 2) + (t & 3)) * 4, __builtin_bit_cast(int, d)));
  float mean = rs.x * (1.f / 512.f);
  float var = __builtin_fmaf(s2, 1.f / 512.f, -mean * mean);
  var = (var > 0.f ? var : 0.f) + 1e-5f;
  float rstd = __builtin_amdgcn_rsqf(var);
  f32x4 r = {mean, -2.302208198f * rstd, -0.1029432397f * rstd * rstd * rstd, var * rstd};
  return r;
}
FI void lds_wait_gt(unsigned addr, unsigned v) {
#pragma nounroll
  for (unsigned s = 0; s < SPIN_CAP * 16u; s++) {
    unsigned x = *(volatile LAS unsigned*)addr;
    if ((unsigned)__builtin_amdgcn_readfirstlane(x) > v) break;
  }
  asm volatile("" ::: "memory");
}
FI void lds_set(unsigned addr, unsigned v) {
  asm volatile("" ::: "memory");
  *(volatile LAS unsigned*)addr = v;
  asm volatile("" ::: "memory");
}
#define TS(i)

__global__ __launch_bounds__(512, 2) void mixer(const void* __restrict__ px, const void* __restrict__ pw1, const void* __restrict__ pw2,
                                                const void* __restrict__ pw3, const void* __restrict__ pw4, void* pout, void* pws) {
  const int lane = threadIdx.x & 63;
  const unsigned w = __builtin_amdgcn_readfirstlane(threadIdx.x >> 6);
  const unsigned b = blockIdx.x, m = (b >> 3) & 7, gi = b >> 6, group = (b & 7) * 4 + gi, row0 = group * 64;
  const unsigned rq = w >> 1, hf = w & 1;
  const unsigned t = lane & 15, q = lane >> 4, r4 = (lane >> 2) & 3, p4 = lane & 3;
  const srd_t sx = __builtin_amdgcn_make_buffer_rsrc((void*)px, 0, 2048 * 1024, 0x00020000);
  const srd_t sw1 = __builtin_amdgcn_make_buffer_rsrc((void*)pw1, 0, 512 * 1024, 0x00020000);
  const srd_t sw2 = __builtin_amdgcn_make_buffer_rsrc((void*)pw2, 0, 512 * 1024, 0x00020000);
  const srd_t sw3 = __builtin_amdgcn_make_buffer_rsrc((void*)pw3, 0, 512 * 3584, 0x00020000);
  const srd_t sw4 = __builtin_amdgcn_make_buffer_rsrc((void*)pw4, 0, 1792 * 1024, 0x00020000);
  const srd_t so = __builtin_amdgcn_make_buffer_rsrc(pout, 0, 2048 * 1024, 0x00020000);
  const srd_t sws = __builtin_amdgcn_make_buffer_rsrc(pws, 0, WS_END, 0x00020000);
  const unsigned long long epoch = *(const unsigned long long*)((const char*)pws + WS_EP + group * 128);
  const unsigned l16 = lane * 16;
  const v4u SENT = {~0u, ~0u, ~0u, ~0u};
  const unsigned hb = WS_H + group * 65536u, xb = WS_X1 + group * 65536u, gb = WS_G + group * 229376u;
  const unsigned myfrag = (rq * 16 + 2 * m + hf) * K1;

  st4_sc1(SENT, sws, l16, hb + myfrag);
  {
    const int cg = m >> 1;
    const int dl = (cg & 1) ? (cg >> 1) + 1 : -((cg >> 1) + 1);
    const int ra = (int)row0 + dl + 4 * (int)w + (int)q, rb = ra + 32;
    const unsigned xc = (t ^ ((4 * w + q) & 15)) * 16;
    const unsigned vxa = (ra >= 0 && ra < 2048) ? ra * 1024 + xc : OOB;
    const unsigned vxb = (rb >= 0 && rb < 2048) ? rb * 1024 + xc : OOB;
    const unsigned g1 = (q & 1) | ((w & 1) << 1);
    const unsigned vw1 = (8 * w + (lane >> 3)) * 1024 + 128 * m + (((((lane & 7) >> 1) ^ g1) << 1) | (lane & 1)) * 16;
#pragma unroll
    for (int i = 0; i < 4; i++) {
      const unsigned ci = (i + gi + m) & 3, L = ((i + 3) & 3) * 32 * K1;
      dma(sx, vxa, ci * 256, L + w * K1);
      dma(sx, vxb, ci * 256, L + (w + 8) * K1);
      dma(sw1, vw1, ci * 128 * K1, L + (16 + w) * K1);
      dma(sw1, vw1, ci * 128 * K1 + 64 * K1, L + (24 + w) * K1);
    }
  }
  const unsigned vw2 = (lane >> 3) * 1024 + 128 * m + (((((lane & 7) >> 1) ^ q) << 1) | (lane & 1)) * 16;
#pragma unroll
  for (int j = 0; j < 4; j++) {
    const unsigned i = w + 8 * ((j + gi) & 3);
    dma(sw2, vw2, i * 8 * K1, 128 * K1 + i * K1);
  }
  st4_sc1(SENT, sws, l16, xb + myfrag);
#pragma unroll
  for (int c = 0; c < 4; c++) st4_sc1(SENT, sws, l16, w < 7 ? gb + (c * 56 + 7 * m + w) * K1 : xb + myfrag);
  v4u xres;
  const unsigned vrow = (row0 + 16 * rq + t) * 1024 + 128 * m + 64 * hf + (q & 1) * 32 + (q >> 1) * 16;
  ld4(xres, sx, vrow, 0);
  const unsigned long long tag64 = epoch * 6364136223846793005ull + 1442695040888963407ull;
  const v2u tag = {(unsigned)tag64, (unsigned)(tag64 >> 32)};
  const unsigned flA = WS_FL + group * 1024, flB = flA + 512;
  const unsigned vflag = lane == 0 ? (m * 8 + w) * 8 : OOB;
  unsigned vw3;
  {
    const unsigned rr = 2 * w + (lane >> 5), c = lane & 31;
    const unsigned wb = (((c >> 1) ^ (rr & 7)) << 5) | ((c & 1) << 4), lo = 64 * (m & 1);
    vw3 = (wb >= lo && wb < lo + 448) ? rr * 3584 + 448 * m - lo + wb : OOB;
  }
  auto w3dma = [&](int s, unsigned L) {
    const unsigned ks = (s + 4 * gi) & 15;
    dma(sw3, vw3, ks * 32 * 3584, L + w * K1);
    dma(sw3, vw3, ks * 32 * 3584 + 16 * 3584, L + (w + 8) * K1);
  };
  TS(1);

  const bf16x8 ones = __builtin_bit_cast(bf16x8, (v4u){0x3F803F80u, 0x3F803F80u, 0x3F803F80u, 0x3F803F80u});
  v2u fa;
  {
    f32x4 a0 = {0, 0, 0, 0}, a1 = a0, c0 = a0, c1 = a0, rs = a0, sq = a0;
    const unsigned ax = (16 * rq + t) * 256;
    const unsigned aw = 16 * K1 + (8 * q + r4) * 128 + (((2 * hf) ^ ((r4 >> 1) | ((q & 1) << 1))) << 5) + 8 * p4;
#pragma unroll
    for (int i = 0; i < 4; i++) {
      if (i == 0) { vbar<22>(); TS(2); st2_sc1(tag, sws, vflag, flA); }
      if (i == 1) {
        vbar<19>();
#pragma unroll
        for (int j = 0; j < 4; j++) {
          const unsigned ii = w + 8 * ((j + gi) & 3);
          dma(sw2, vw2, (32 + ii) * 8 * K1, 96 * K1 + ii * K1);
        }
        ld2_sc1(fa, sws, lane * 8, flA);
      }
      if (i == 2) { vbar<20>(); w3dma(0, 0); w3dma(1, 16 * K1); }
      if (i == 3) { vbar<20>(); TS(3); w3dma(2, 32 * K1); w3dma(3, 48 * K1); }
      const unsigned L = ((i + 3) & 3) * 32 * K1;
#pragma unroll
      for (int ks = 0; ks < 4; ks++) {
        bf16x8 xf = asb(lds128(L + ax + (((4 * ks + q) ^ t) << 4)));
        bf16x8 f0 = trfrag(L + aw + ks * 4096, 512), f1 = trfrag(L + (aw ^ 32) + ks * 4096, 512);
        a0 = mfma(f0, xf, a0); a1 = mfma(f1, xf, a1);
        rs = mfma(ones, xf, rs); sq = mfma(xf, xf, sq);
        c0 = mfma(f0, ones, c0); c1 = mfma(f1, ones, c1);
      }
    }
    f32x4 st = row_stats(rs, sq, lane);
    v4u hv = gelu_frag(a0, a1, c0, c1, st);
    st4_sc1(hv, sws, l16, hb + myfrag);
  }
  TS(4);
  vwait<9>(); tie2(fa);
#pragma nounroll
  for (unsigned s = 0; s < SPIN_CAP && __builtin_amdgcn_ballot_w64(fa.x != tag.x || fa.y != tag.y) != 0; s++) {
    ld2_sc1(fa, sws, lane * 8, flA); vwait<0>(); tie2(fa);
  }
  st2_sc1(tag, sws, vflag, flB);
  v4u hfr[8];
  const unsigned hsrc = hb + (rq * 16 + 8 * hf) * K1;
  auto hload = [&]() {
    ld4_sc1<0>(hfr[0], sws, l16, hsrc); ld4_sc1<1024>(hfr[1], sws, l16, hsrc); ld4_sc1<2048>(hfr[2], sws, l16, hsrc); ld4_sc1<3072>(hfr[3], sws, l16, hsrc);
    ld4_sc1<0>(hfr[4], sws, l16, hsrc + 4096); ld4_sc1<1024>(hfr[5], sws, l16, hsrc + 4096); ld4_sc1<2048>(hfr[6], sws, l16, hsrc + 4096); ld4_sc1<3072>(hfr[7], sws, l16, hsrc + 4096);
  };
  hload();
  if (m == 0 && w == 0) st2_sc1(tag, sws, lane == 0 ? 0 : OOB, WS_EP + group * 128); else st2_sc1(tag, sws, OOB, 0);
  bf16x8 w3[16][2];
  f32x4 cs0 = {0, 0, 0, 0}, cs1 = cs0;
  const unsigned a3 = (4 * q + r4) * 512 + (((2 * (m & 1) + 2 * w) ^ ((4 * q + r4) & 7)) << 5) + 8 * p4;
  auto w3reg = [&](int s, unsigned L) {
    w3[s][0] = trfrag(L + a3, 8192); w3[s][1] = trfrag(L + (a3 ^ 32), 8192);
    cs0 = mfma(w3[s][0], ones, cs0); cs1 = mfma(w3[s][1], ones, cs1);
  };
  vbar<11>();
  TS(5);
  w3dma(4, 64 * K1);
  if (w < 7) { w3reg(0, 0); w3reg(1, 16 * K1); w3reg(2, 32 * K1); w3reg(3, 48 * K1); }
  bar();
  w3dma(5, 0); w3dma(6, 16 * K1); w3dma(7, 32 * K1); w3dma(8, 48 * K1);
  v2u fb;
  ld2_sc1(fb, sws, lane * 8, flB);
  TS(6);
  vwait<12>();
#pragma nounroll
  for (unsigned s = 0; s < SPIN_CAP; s++) {
    unsigned mx = 0;
#pragma unroll
    for (int i = 0; i < 8; i++) { tie(hfr[i]); mx = max4(mx, hfr[i]); }
    if (__builtin_amdgcn_ballot_w64(mx == ~0u) == 0) break;
    hload(); vwait<0>();
  }
  TS(7);
  const unsigned g2 = ((q & 1) << 1) | (r4 >> 1);
  const unsigned aw2 = (4 * q + r4) * 128 + 8 * p4;
  v4u x1v;
  {
    f32x4 a[4] = {};
    const unsigned L = 128 * K1 - hf * 32 * K1 + aw2;
#pragma unroll
    for (int i = 0; i < 8; i++)
#pragma unroll
      for (int j = 0; j < 4; j++) a[j] = mfma(trfrag(L + (((j ^ (2 * hf)) ^ g2) << 5) + i * 4096, 2048), asb(hfr[i]), a[j]);
    *(LAS f32x4*)(L_EX + w * 2048 + l16) = a[2];
    *(LAS f32x4*)(L_EX + w * 2048 + 1024 + l16) = a[3];
    bar();
    w3dma(14, 128 * K1); w3dma(15, 144 * K1);
    a[0] += *(LAS f32x4*)(L_EX + (w ^ 1) * 2048 + l16);
    a[1] += *(LAS f32x4*)(L_EX + (w ^ 1) * 2048 + 1024 + l16);
    tie(xres);
    auto s0 = __builtin_amdgcn_permlane16_swap(xres.x, xres.z, false, false);
    auto s1 = __builtin_amdgcn_permlane16_swap(xres.y, xres.w, false, false);
    x1v.x = umin(pk(a[0].x + blo(s0[0]), a[0].y + bhi(s0[0])), 0xFFFFFFFEu);
    x1v.y = umin(pk(a[0].z + blo(s1[0]), a[0].w + bhi(s1[0])), 0xFFFFFFFEu);
    x1v.z = umin(pk(a[1].x + blo(s0[1]), a[1].y + bhi(s0[1])), 0xFFFFFFFEu);
    x1v.w = umin(pk(a[1].z + blo(s1[1]), a[1].w + bhi(s1[1])), 0xFFFFFFFEu);
    st4_sc1(x1v, sws, l16, xb + myfrag);
  }
  TS(8);
  vbar<5>();
  tie2(fb);
  if (w < 7) { w3reg(4, 64 * K1); w3reg(5, 0); w3reg(6, 16 * K1); w3reg(7, 32 * K1); w3reg(8, 48 * K1); }
  else if (lane < 8) *(volatile LAS unsigned*)(L_FLG + lane * 4) = 0;
#pragma nounroll
  for (unsigned s = 0; s < SPIN_CAP && __builtin_amdgcn_ballot_w64(fb.x != tag.x || fb.y != tag.y) != 0; s++) {
    ld2_sc1(fb, sws, lane * 8, flB); vwait<0>(); tie2(fb);
  }
  bar();
  TS(9);
  w3dma(9, 0); w3dma(10, 16 * K1); w3dma(11, 32 * K1); w3dma(12, 48 * K1); w3dma(13, 64 * K1);

  bf16x8 w4[7][4];
  const unsigned stg = w * 12 * K1;
  auto w4dma = [&](int s, unsigned L) {
    unsigned ks = s + 2 * gi; ks = ks >= 7 ? ks - 7 : ks;
#pragma unroll
    for (int i = 0; i < 4; i++) dma(sw4, vw2, (224 * w + 32 * ks + 8 * i) * K1, L + i * K1);
  };
  auto w4reg = [&](int s, unsigned L) {
#pragma unroll
    for (int j = 0; j < 4; j++) w4[s][j] = trfrag(L + aw2 + ((j ^ g2) << 5), 2048);
  };
  v4u gf[3][7];
  auto gload = [&](int c) {
#pragma unroll
    for (int s = 0; s < 7; s++) {
      unsigned ks = s + 2 * gi; ks = ks >= 7 ? ks - 7 : ks;
      ld4_sc1<0>(gf[c % 3][s], sws, l16, gb + (c * 56 + 7 * w + ks) * K1);
    }
  };
  if (w == 7) {
    auto xdma = [&](unsigned c) {
#pragma unroll
      for (int i = 0; i < 16; i++) dma_sc1(sws, l16, xb + (c * 16 + i) * K1, L_X1 + (c * 16 + i) * K1);
    };
    v4u f[16];
    auto xcheck = [&](unsigned c) {
      unsigned mx = 0;
#pragma unroll
      for (int i = 0; i < 16; i++) { f[i] = lds128(L_X1 + (c * 16 + i) * K1 + l16); mx = max4(mx, f[i]); }
      return __builtin_amdgcn_ballot_w64(mx == ~0u) == 0;
    };
    xdma(0);
    vbar<16>();
    vwait<0>();
    bool ok = xcheck(0);
    if (ok) { xdma(1); lds_set(L_FLG, 1); }
    bar();
    TS(10);
    if (!ok) {
#pragma nounroll
      for (unsigned s = 0; s < SPIN_CAP && !ok; s++) {
        xdma(0); vwait<0>(); ok = xcheck(0);
      }
      xdma(1); lds_set(L_FLG, 1);
    }
    xdma(2); xdma(3);
    w4dma(0, stg); w4dma(1, stg + 4096);
    TS(11);
#pragma nounroll
    for (unsigned c = 0; c < 4; c++) {
      if (c > 0) {
        if (c == 1) vwait<40>(); else if (c == 2) vwait<24>(); else vwait<8>();
        ok = xcheck(c);
#pragma nounroll
        for (unsigned s = 0; s < SPIN_CAP && !ok; s++) {
          xdma(c); vwait<0>(); ok = xcheck(c);
        }
        lds_set(L_FLG, c + 1);
      }
      f32x4 rs = {0, 0, 0, 0}, sq = rs;
#pragma unroll
      for (int i = 0; i < 16; i++) { rs = mfma(ones, asb(f[i]), rs); sq = mfma(asb(f[i]), asb(f[i]), sq); }
      f32x4 st = row_stats(rs, sq, lane);
      if (lane < 16) *(LAS f32x4*)(L_ST + (c * 16 + lane) * 16) = st;
      lds_set(L_FLG + 4, c + 1);
    }
    TS(12);
    vwait<0>();
    w4reg(0, stg); w4reg(1, stg + 4096);
    asm volatile("s_waitcnt lgkmcnt(0)" ::: "memory");
    w4dma(2, stg); w4dma(3, stg + 4096);
    vwait<0>();
    w4reg(2, stg); w4reg(3, stg + 4096);
    asm volatile("s_waitcnt lgkmcnt(0)" ::: "memory");
    w4dma(4, stg); w4dma(5, stg + 4096);
    lds_wait_gt(L_FLG + 8, 6);
    w4dma(6, L_X1 + w * 4096);
    gload(0); gload(1);
    vwait<14>();
    w4reg(4, stg); w4reg(5, stg + 4096); w4reg(6, L_X1 + w * 4096);
    TS(13); TS(14); TS(15);
  } else {
    vbar<0>();
    w3reg(9, 0); w3reg(10, 16 * K1); w3reg(11, 32 * K1); w3reg(12, 48 * K1); w3reg(13, 64 * K1); w3reg(14, 128 * K1); w3reg(15, 144 * K1);
    bar();
    TS(10);
    unsigned xa[4];
#pragma unroll
    for (int i = 0; i < 4; i++) { xa[i] = L_X1 + ((i + gi) & 3) * 4096 + l16; asm volatile("" : "+v"(xa[i])); }
#pragma unroll
    for (int c = 0; c < 4; c++) {
      lds_wait_gt(L_FLG, c);
      if (c == 0) { TS(11); w4dma(0, stg); w4dma(1, stg + 4096); w4dma(2, stg + 8192); }
      f32x4 a0 = {0, 0, 0, 0}, a1 = a0;
#pragma unroll
      for (int s = 0; s < 16; s++) {
        bf16x8 xf = asb(lds128(xa[s >> 2] + c * 16 * K1 + (s & 3) * K1));
        a0 = mfma(w3[s][0], xf, a0); a1 = mfma(w3[s][1], xf, a1);
      }
      if (c == 1) {
        asm volatile("" ::: "memory");
        if (lane == 0) __hip_atomic_fetch_add((LAS unsigned*)(L_FLG + 8), 1u, __ATOMIC_RELAXED, __HIP_MEMORY_SCOPE_WORKGROUP);
        vwait<1>();
        w4reg(0, stg); w4reg(1, stg + 4096); w4reg(2, stg + 8192);
        asm volatile("s_waitcnt lgkmcnt(0)" ::: "memory");
        w4dma(3, stg); w4dma(4, stg + 4096); w4dma(5, stg + 8192);
      }
      if (c == 2) { lds_wait_gt(L_FLG + 8, 6); w4dma(6, L_X1 + w * 4096); }
      if (c == 3) { gload(0); gload(1); }
      lds_wait_gt(L_FLG + 4, c);
      f32x4 st = *(LAS f32x4*)(L_ST + (c * 16 + t) * 16);
      v4u gv = gelu_frag(a0, a1, cs0, cs1, st);
      st4_sc1(gv, sws, l16, gb + (c * 56 + 7 * m + w) * K1);
      TS(12 + c);
    }
    vwait<15>();
    w4reg(3, stg); w4reg(4, stg + 4096); w4reg(5, stg + 8192); w4reg(6, L_X1 + w * 4096);
  }
  asm volatile("s_waitcnt lgkmcnt(0)" ::: "memory");
  TS(16);
#pragma unroll
  for (int c = 0; c < 4; c++) {
    if (c + 2 < 4) gload(c + 2);
    if (c == 0) vwait<14>(); else if (c == 1) vwait<14>(); else if (c == 2) vwait<7>(); else vwait<0>();
#pragma nounroll
    for (unsigned s = 0; s < SPIN_CAP; s++) {
      unsigned mx = 0;
#pragma unroll
      for (int i = 0; i < 7; i++) { tie(gf[c % 3][i]); mx = max4(mx, gf[c % 3][i]); }
      if (__builtin_amdgcn_ballot_w64(mx == ~0u) == 0) break;
      gload(c); vwait<0>();
    }
    f32x4 a[4] = {};
#pragma unroll
    for (int s = 0; s < 7; s++)
#pragma unroll
      for (int j = 0; j < 4; j++) a[j] = mfma(w4[s][j], asb(gf[c % 3][s]), a[j]);
#pragma unroll
    for (int j = 0; j < 4; j++) *(LAS f32x4*)(stg + (c & 1) * 4096 + j * K1 + l16) = a[j];
    bar();
    if (rq == c) {
      f32x4 o0 = {blo(x1v.x), bhi(x1v.x), blo(x1v.y), bhi(x1v.y)}, o1 = {blo(x1v.z), bhi(x1v.z), blo(x1v.w), bhi(x1v.w)};
      const unsigned ra = (c & 1) * 4096 + hf * 2048 + l16;
#pragma unroll
      for (int p = 0; p < 8; p++) { o0 += *(LAS f32x4*)(p * 12 * K1 + ra); o1 += *(LAS f32x4*)(p * 12 * K1 + ra + 1024); }
      auto s0 = __builtin_amdgcn_permlane16_swap(pk(o0.x, o0.y), pk(o1.x, o1.y), false, false);
      auto s1 = __builtin_amdgcn_permlane16_swap(pk(o0.z, o0.w), pk(o1.z, o1.w), false, false);
      v4u ov = {s0[0], s1[0], s0[1], s1[1]};
      st4(ov, so, vrow, 0);
    }
    TS(17 + c);
  }
}

extern "C" void kernel_launch(void* const* d_in, const int* in_sizes, int n_in, void* d_out, int out_size, void* d_ws, size_t ws_size,
                              hipStream_t stream) {
  (void)hipFuncSetAttribute((const void*)mixer, hipFuncAttributeMaxDynamicSharedMemorySize, L_TOTAL);
  mixer<<<256, 512, L_TOTAL, stream>>>(d_in[0], d_in[1], d_in[2], d_in[3], d_in[4], d_out, d_ws);
}
```

```cpp
#include <hip/hip_runtime.h>
#define LAS __attribute__((address_space(3)))
typedef __bf16 bf16x8 __attribute__((ext_vector_type(8)));
typedef __bf16 bf16x2 __attribute__((ext_vector_type(2)));
typedef short s16x4 __attribute__((ext_vector_type(4)));
typedef short s16x8 __attribute__((ext_vector_type(8)));
typedef float f32x4 __attribute__((ext_vector_type(4)));
typedef float f32x2 __attribute__((ext_vector_type(2)));
typedef unsigned v4u __attribute__((ext_vector_type(4)));
typedef unsigned v2u __attribute__((ext_vector_type(2)));
typedef __amdgpu_buffer_rsrc_t srd_t;
typedef unsigned long long u64;

constexpr unsigned K1 = 1024u, CELL = 16 * K1;
constexpr unsigned WS_EP = 0, WS_FL = 8192, WS_H = 65536, WS_X1 = WS_H + 32u * 65536u, WS_G = WS_X1 + 32u * 65536u,
                   WS_END = WS_G + 32u * 229376u;
constexpr unsigned L_X1 = 96 * K1, L_ST1 = 78 * K1, L_CS = 79 * K1, L_ST3 = 94 * K1, L_FLG = 95 * K1, L_TOTAL = 160 * K1;
constexpr unsigned OOB = 0x80000000u;
#define SPINC 200000u
constexpr unsigned SPIN_CAP = SPINC;

#define EXP 0
#define ROT 1
#define FI __device__ __forceinline__
#define SB() __builtin_amdgcn_sched_barrier(0)
FI void dma(srd_t s, unsigned voff, unsigned soff, unsigned lds) {
  asm volatile("s_mov_b32 m0, %0\n\ts_nop 0\n\tbuffer_load_dwordx4 %1, %2, %3 offen lds" ::"s"(lds), "v"(voff), "s"(s), "s"(soff) : "memory");
}
FI void dma56x2(srd_t s, unsigned voff, unsigned soff0, unsigned soff1, unsigned lds0, unsigned lds1) {
  asm volatile("s_mov_b32 m0, %0\n\ts_mov_b32 exec_hi, 0xffffff\n\tbuffer_load_dwordx4 %2, %3, %4 offen lds\n\t"
               "s_mov_b32 m0, %1\n\ts_nop 0\n\tbuffer_load_dwordx4 %2, %3, %5 offen lds\n\ts_mov_b32 exec_hi, -1"
               ::"s"(lds0), "s"(lds1), "v"(voff), "s"(s), "s"(soff0), "s"(soff1) : "memory");
}
FI void dma_sc1(srd_t s, unsigned voff, unsigned soff, unsigned lds) {
  asm volatile("s_mov_b32 m0, %0\n\ts_nop 0\n\tbuffer_load_dwordx4 %1, %2, %3 offen sc1 lds" ::"s"(lds), "v"(voff), "s"(s), "s"(soff) : "memory");
}
template <int OFF> FI void ld4_sc1(v4u& d, srd_t s, unsigned voff, unsigned soff) {
  asm volatile("buffer_load_dwordx4 %0, %1, %2, %3 offen offset:%4 sc1" : "=v"(d) : "v"(voff), "s"(s), "s"(soff), "n"(OFF) : "memory");
}
FI void reld2_sc1(v4u& d0, v4u& d1, srd_t s, unsigned voff, unsigned soff) {
  asm volatile("buffer_load_dwordx4 %0, %2, %3, %4 offen sc1\n\tbuffer_load_dwordx4 %1, %2, %3, %4 offen offset:1024 sc1\n\ts_waitcnt vmcnt(0)"
               : "+v"(d0), "+v"(d1) : "v"(voff), "s"(s), "s"(soff) : "memory");
}
template <int OFF> FI void ld4(v4u& d, srd_t s, unsigned voff, unsigned soff) {
  asm volatile("buffer_load_dwordx4 %0, %1, %2, %3 offen offset:%4" : "=v"(d) : "v"(voff), "s"(s), "s"(soff), "n"(OFF) : "memory");
}
FI void ld2_sc1(u64& d, srd_t s, unsigned voff, unsigned soff) {
  asm volatile("buffer_load_dwordx2 %0, %1, %2, %3 offen sc1" : "=v"(d) : "v"(voff), "s"(s), "s"(soff) : "memory");
}
FI void st4_sc1(v4u d, srd_t s, unsigned voff, unsigned soff) {
  asm volatile("buffer_store_dwordx4 %0, %1, %2, %3 offen sc1\n\ts_nop 1" ::"v"(d), "v"(voff), "s"(s), "s"(soff) : "memory");
}
FI void st1(unsigned d, srd_t s, unsigned voff, unsigned soff) {
  asm volatile("buffer_store_dword %0, %1, %2, %3 offen" ::"v"(d), "v"(voff), "s"(s), "s"(soff) : "memory");
}
FI void st2_sc1(u64 d, srd_t s, unsigned voff, unsigned soff) {
  asm volatile("buffer_store_dwordx2 %0, %1, %2, %3 offen sc1" ::"v"(d), "v"(voff), "s"(s), "s"(soff) : "memory");
}
template <int N> FI void vwait() { asm volatile("s_waitcnt vmcnt(%0)" ::"n"(N) : "memory"); }
template <int N> FI void lwait() { asm volatile("s_waitcnt lgkmcnt(%0)" ::"n"(N) : "memory"); }
FI void bar() { asm volatile("s_waitcnt lgkmcnt(0)\n\ts_barrier" ::: "memory"); }
template <class T> FI void tie(T& a) { asm volatile("" : "+v"(a)); }
struct Fr { s16x4 lo, hi; };
FI void tie(Fr& f) { asm volatile("" : "+v"(f.lo), "+v"(f.hi)); }
FI void trr(s16x4& d, unsigned a, int off) { asm volatile("ds_read_b64_tr_b16 %0, %1 offset:%2" : "=v"(d) : "v"(a), "i"(off) : "memory"); }
FI void frr(Fr& f, unsigned a, int off, int sec) { trr(f.lo, a, off); trr(f.hi, a, off + sec); }
template <class T> FI void lr4(T& d, unsigned a, int off) { asm volatile("ds_read_b128 %0, %1 offset:%2" : "=v"(d) : "v"(a), "i"(off) : "memory"); }
template <class T> FI void lr2(T& d, unsigned a, int off) { asm volatile("ds_read_b64 %0, %1 offset:%2" : "=v"(d) : "v"(a), "i"(off) : "memory"); }
FI void vwaitn(int n) { asm volatile("s_waitcnt vmcnt(%0)" ::"i"(n) : "memory"); }
FI void lwaitn(int n) { asm volatile("s_waitcnt lgkmcnt(%0)" ::"i"(n) : "memory"); }
FI bf16x8 fv(const Fr& f) { return __builtin_bit_cast(bf16x8, __builtin_shufflevector(f.lo, f.hi, 0, 1, 2, 3, 4, 5, 6, 7)); }
FI f32x4 mfma(bf16x8 a, bf16x8 b, f32x4 c) { return __builtin_amdgcn_mfma_f32_16x16x32_bf16(a, b, c, 0, 0, 0); }
FI bf16x8 asb(v4u v) { return __builtin_bit_cast(bf16x8, v); }
FI unsigned pk(float a, float b) {
  f32x2 f = {a, b};
  return __builtin_bit_cast(unsigned, __builtin_convertvector(f, bf16x2));
}
FI unsigned umin(unsigned a, unsigned b) { return a < b ? a : b; }
FI unsigned umax(unsigned a, unsigned b) { return a > b ? a : b; }
FI unsigned max4(unsigned m, v4u v) { return umax(umax(umax(m, v.x), v.y), umax(v.z, v.w)); }
FI bool anyset(unsigned mx) { return __builtin_amdgcn_ballot_w64(mx == ~0u) != 0; }
FI float blo(unsigned u) { return __builtin_bit_cast(float, u << 16); }
FI float bhi(unsigned u) { return __builtin_bit_cast(float, u & 0xffff0000u); }
FI unsigned opq(unsigned a) { asm volatile("" : "+v"(a)); return a; }
FI float gelu_n(float a, float k1, float k2, float irs) {
  float u = __builtin_fmaf(a * a, k2, k1);
  float e = __builtin_amdgcn_exp2f(a * u);
  return a * __builtin_amdgcn_rcpf(__builtin_fmaf(e, irs, irs));
}
FI v4u gelu_frag(f32x4 a0, f32x4 a1, f32x4 c0, f32x4 c1, f32x4 st) {
  float o[8];
#pragma unroll
  for (int j = 0; j < 4; j++) {
    o[j] = gelu_n(__builtin_fmaf(-st.x, c0[j], a0[j]), st.y, st.z, st.w);
    o[4 + j] = gelu_n(__builtin_fmaf(-st.x, c1[j], a1[j]), st.y, st.z, st.w);
  }
  v4u r = {pk(o[0], o[1]), pk(o[2], o[3]), pk(o[4], o[5]), pk(o[6], o[7])};
  return r;
}
FI f32x4 mkstats(float s1, float s2) {
  float mean = s1 * (1.f / 512.f);
  float var = __builtin_fmaf(s2, 1.f / 512.f, -mean * mean);
  var = (var > 0.f ? var : 0.f) + 1e-5f;
  float rstd = __builtin_amdgcn_rsqf(var);
  f32x4 r = {mean, -2.302208198f * rstd, -0.1029432397f * rstd * rstd * rstd, var * rstd};
  return r;
}
FI float diag(f32x4 sq, unsigned t) { return (t & 2) ? ((t & 1) ? sq.w : sq.z) : ((t & 1) ? sq.y : sq.x); }
FI void lds_wait_gt(unsigned addr, unsigned v) {
  unsigned x, n;
  asm volatile("s_mov_b32 %1, %4\n1:\n\tds_read_b32 %0, %2\n\ts_waitcnt lgkmcnt(0)\n\tv_cmp_lt_u32 vcc, %3, %0\n\ts_cbranch_vccnz 2f\n\t"
               "s_sub_u32 %1, %1, 1\n\ts_cbranch_scc0 1b\n2:"
               : "=&v"(x), "=&s"(n) : "v"(addr), "i"(v), "i"(SPIN_CAP * 16u) : "memory", "vcc", "scc");
}
FI unsigned chkflag(u64& f, srd_t s, unsigned voff, unsigned soff, u64 lo, u64 hi, u64 mask) {
  unsigned n; u64 tmp;
  asm volatile("s_mov_b32 %1, %9\n1:\n\tv_cmp_gt_u64 vcc, %6, %0\n\tv_cmp_lt_u64 %2, %7, %0\n\ts_or_b64 vcc, vcc, %2\n\t"
               "s_and_b64 vcc, vcc, %8\n\ts_cbranch_scc0 2f\n\tbuffer_load_dwordx2 %0, %3, %4, %5 offen sc1\n\ts_waitcnt vmcnt(0)\n\t"
               "s_sub_u32 %1, %1, 1\n\ts_cbranch_scc0 1b\n2:"
               : "+v"(f), "=&s"(n), "=&s"(tmp) : "v"(voff), "s"(s), "s"(soff), "s"(lo), "s"(hi), "s"(mask), "i"(SPIN_CAP) : "memory", "vcc", "scc");
  return SPIN_CAP - n;
}
FI void lds_set(unsigned addr, unsigned v) { asm volatile("ds_write_b32 %0, %1" ::"v"(addr), "v"(v) : "memory"); }
#define TS(i)
#define RT(i, n)
FI void vwait2(unsigned w, int a, int b) {
  asm volatile("s_waitcnt vmcnt(%2)\n\ts_cmp_eq_u32 %0, 7\n\ts_cbranch_scc1 1f\n\ts_waitcnt vmcnt(%1)\n1:" ::"s"(w), "i"(a), "i"(b) : "memory", "scc");
}

__global__ __launch_bounds__(512, 2) void mixer(const void* __restrict__ px, const void* __restrict__ pw1, const void* __restrict__ pw2,
                                                const void* __restrict__ pw3, const void* __restrict__ pw4, void* pout, void* pws) {
  const unsigned lane = threadIdx.x & 63;
  const unsigned w = __builtin_amdgcn_readfirstlane(threadIdx.x >> 6);
  const bool w7 = w == 7;
  const unsigned b = blockIdx.x, m = (b >> 3) & 7, gi = b >> 6, group = (b & 7) * 4 + gi, row0 = group * 64;
  const unsigned rq = w >> 1, hf = w & 1;
  const unsigned t = lane & 15, q = lane >> 4, r4 = (lane >> 2) & 3, p4 = lane & 3;
  const srd_t sx = __builtin_amdgcn_make_buffer_rsrc((void*)px, 0, 2048 * 1024, 0x00020000);
  const srd_t sw1 = __builtin_amdgcn_make_buffer_rsrc((void*)pw1, 0, 512 * 1024, 0x00020000);
  const srd_t sw2 = __builtin_amdgcn_make_buffer_rsrc((void*)pw2, 0, 512 * 1024, 0x00020000);
  const srd_t sw3 = __builtin_amdgcn_make_buffer_rsrc((void*)pw3, 0, 512 * 3584, 0x00020000);
  const srd_t sw4 = __builtin_amdgcn_make_buffer_rsrc((void*)pw4, 0, 1792 * 1024, 0x00020000);
  const srd_t so = __builtin_amdgcn_make_buffer_rsrc(pout, 0, 2048 * 1024, 0x00020000);
  const srd_t sws = __builtin_amdgcn_make_buffer_rsrc(pws, 0, WS_END, 0x00020000);
  const u64 epoch = *(const u64*)((const char*)pws + WS_EP + group * 128);
  const unsigned l16 = lane * 16;
  const unsigned hb = WS_H + group * 65536u, xb = WS_X1 + group * 65536u, gb = WS_G + group * 229376u;
  const unsigned myfrag = (rq * 16 + 2 * m + hf) * K1;
  const unsigned rot = ROT ? gi : 0;

  const unsigned rho = lane >> 3;
  const unsigned wsw = 128 * m + (((((lane & 7) >> 1) ^ q) << 1) | (lane & 1)) * 16;
  const unsigned vw2 = (8 * w + rho) * 1024 + wsw;
  const unsigned vw1 = (32 * (w >> 2) + 16 * (w & 1) + 4 * ((w >> 1) & 1) + 8 * (rho >> 2) + (rho & 3)) * 1024 + wsw;
  const unsigned xsw = ((lane & 7) ^ (4 * hf + (rho >> 1))) * 16;
  unsigned vx;
  {
    const int cg = m >> 1;
    const int dl = (cg & 1) ? (cg >> 1) + 1 : -((cg >> 1) + 1);
    const int ra = (int)(row0 + 8 * w + rho) + dl;
    vx = (ra >= 0 && ra < 2048) ? ra * 1024 + xsw : OOB;
  }
  auto p1dma = [&](int p, int i) {
    const unsigned pp = (p + rot + (ROT ? m : 0)) & 3;
    if (i < 2) dma(sw1, vw1, pp * 64 * K1 + i * 256 * K1, p * CELL + i * 8 * K1 + w * K1);
    else dma(sx, vx, pp * 128 + (i - 2) * 512, (p < 2 ? 8 + p : 2 + p) * CELL + (i - 2) * 8 * K1 + w * K1);
  };
#pragma unroll
  for (int p = 0; p < 3; p++) { p1dma(p, 0); p1dma(p, 2); p1dma(p, 1); p1dma(p, 3); }
  auto w2dma = [&](int p, int h2) { dma(sw2, vw2, (((p + rot) & 3) * 64 + h2 * 256) * K1, L_X1 + p * CELL + h2 * 8 * K1 + w * K1); };
  const unsigned vw3 = lane < 28 ? 4 * w * 3584 + 448 * m + (lane ^ (2 * hf)) * 16 : (4 * w + 1) * 3584 + 448 * m + ((lane - 28) ^ (2 * hf)) * 16;
  auto w3dma = [&](int s, int cell) {
    const unsigned so3 = ((s + 4 * rot) & 15) * 32 * 3584;
    dma56x2(sw3, vw3, so3, so3 + 2 * 3584, cell * CELL + w * 1792, cell * CELL + w * 1792 + 896);
  };
  const u64 tag = (epoch * 6364136223846793005ull + 1442695040888963407ull) & ~7ull;
  const unsigned flA = WS_FL + group * 1024, flG = flA + 512;
  const unsigned vflag = lane == 0 ? (m * 8 + w) * 8 : OOB;
  const unsigned g2 = ((q & 1) << 1) | (r4 >> 1);
  unsigned af[4];
#pragma unroll
  for (int j = 0; j < 4; j++) af[j] = opq((4 * q + r4) * 128 + 8 * p4 + (((j ^ (2 * hf)) ^ g2) << 5) + hf * 8 * K1);
  const unsigned aex = opq(2 * CELL + w * 2 * K1 + l16), aexp = opq(2 * CELL + (w ^ 1) * 2 * K1 + l16);
  const unsigned rtile = (rq + (rq >> 1) + (rq == 3 ? 1 : 0)) * CELL + 14 * K1;
  TS(1);

  const bf16x8 ones = __builtin_bit_cast(bf16x8, (v4u){0x3F803F80u, 0x3F803F80u, 0x3F803F80u, 0x3F803F80u});
  {
    f32x4 a[4] = {}, rs = {0, 0, 0, 0}, sq = rs, cc = rs;
    unsigned ax0 = opq(8 * CELL + hf * 8 * K1 + (16 * rq + t) * 128 + ((q ^ (t >> 1)) << 4)), ax1 = opq(ax0 ^ 64);
#pragma unroll
    for (int p = 0; p < 4; p++) {
      if (p == 0) vwait<8>();
      if (p == 1) vwait<10>();
      if (p == 2) vwait<12>();
      if (p == 3) { vwait<12>(); st2_sc1(tag, sws, vflag, flA); }
      bar();
      if (p == 0) TS(2);
      if (p == 3) TS(3);
      if (p == 1) TS(20);
      if (p == 2) TS(21);
      if (p == 2) { ax0 = opq(ax0 - 4 * CELL); ax1 = opq(ax1 - 4 * CELL); }
      v4u x0, x1;
      lr4(x0, ax0, (p & 1) * CELL); lr4(x1, ax1, (p & 1) * CELL);
      Fr f[8];
#pragma unroll
      for (int i = 0; i < 8; i++) frr(f[i], af[i & 3], p * CELL + (i >> 2) * 4096, 2048);
      if (p == 0) { p1dma(3, 0); p1dma(3, 2); }
      if (p == 1) { w2dma(0, 0); w2dma(0, 1); }
      if (p == 2) { w2dma(3, 0); w2dma(3, 1); }
      if (p == 3) w3dma(2, 4);
      lwait<15>(); tie(x0); tie(x1);
      if (p == 1) TS(19);
      SB();
      rs = mfma(ones, asb(x0), rs); sq = mfma(asb(x0), asb(x0), sq);
      rs = mfma(ones, asb(x1), rs); sq = mfma(asb(x1), asb(x1), sq);
      SB();
#pragma unroll
      for (int g = 0; g < 4; g++) {
        if (p == 0 && g < 2) p1dma(3, 1 + 2 * g);
        if (p == 0 && g >= 2) { const v4u SENT = {~0u, ~0u, ~0u, ~0u}; st4_sc1(SENT, sws, l16, (g == 2 ? hb : xb) + myfrag); }
        if (p == 1) w2dma(1 + (g >> 1), g & 1);
        if (p == 2 && (g & 1) == 0) w3dma(g >> 1, g >> 1);
        lwaitn(12 - 4 * g);
        tie(f[2 * g]); tie(f[2 * g + 1]);
        SB();
        a[(2 * g) & 3] = mfma(fv(f[2 * g]), asb(g < 2 ? x0 : x1), a[(2 * g) & 3]);
        a[(2 * g + 1) & 3] = mfma(fv(f[2 * g + 1]), asb(g < 2 ? x0 : x1), a[(2 * g + 1) & 3]);
        SB();
      }
      if (rq == 0) { cc = mfma(fv(f[0]), ones, cc); cc = mfma(fv(f[4]), ones, cc); }
      else if (rq == 1) { cc = mfma(fv(f[1]), ones, cc); cc = mfma(fv(f[5]), ones, cc); }
      else if (rq == 2) { cc = mfma(fv(f[2]), ones, cc); cc = mfma(fv(f[6]), ones, cc); }
      else { cc = mfma(fv(f[3]), ones, cc); cc = mfma(fv(f[7]), ones, cc); }
    }
    *(LAS f32x4*)(aex) = a[2];
    *(LAS f32x4*)(aex + 1024) = a[3];
    if (q == (t >> 2)) { f32x2 sv = {rs.x, diag(sq, t)}; *(LAS f32x2*)(L_ST1 + w * 128 + t * 8) = sv; }
    if (t == 0) *(LAS f32x4*)(L_CS + hf * 256 + (rq ^ (2 * hf)) * 64 + q * 16) = cc;
    bar();
    TS(4);
    f32x4 pa0, pa1, c00, c01, c10, c11;
    f32x2 s0, s1;
    {
      const unsigned as = L_ST1 + (w & 6) * 128 + t * 8, ac = L_CS + hf * 128 + q * 16;
      lr2(s0, as, 0); lr2(s1, as, 128);
      lr4(pa0, aexp, 0); lr4(pa1, aexp, 1024);
      lr4(c00, ac, 0); lr4(c01, ac, 64); lr4(c10, ac, 256); lr4(c11, ac, 320);
    }
    w3dma(3, 3);
    lwait<6>(); tie(s0); tie(s1);
    f32x4 st = mkstats(s0.x + s1.x, s0.y + s1.y);
    lwait<0>(); tie(pa0); tie(pa1); tie(c00); tie(c01); tie(c10); tie(c11);
    v4u hv = gelu_frag(a[0] + pa0, a[1] + pa1, c00 + c10, c01 + c11, st);
    hv.x = umin(hv.x, 0xFFFFFFFEu); hv.y = umin(hv.y, 0xFFFFFFFEu); hv.z = umin(hv.z, 0xFFFFFFFEu); hv.w = umin(hv.w, 0xFFFFFFFEu);
    st4_sc1(hv, sws, l16, hb + myfrag);
  }
  TS(5);
  w3dma(4, 5);
  dma(sx, (row0 + 8 * w + rho) * 1024 + 128 * m + xsw, 0, rtile + hf * K1);
  vwait<6>();
  bar();
  u64 fa;
  ld2_sc1(fa, sws, lane * 8, flA);
#pragma unroll
  for (int j = 0; j < 4; j++) af[j] = opq(af[j] + L_X1);
  Fr u[2][8];
  auto w2rd = [&](int p) {
#pragma unroll
    for (int i = 0; i < 8; i++) frr(u[p & 1][i], af[i & 3], p * CELL + (i >> 2) * 4096, 2048);
  };
  Fr w3[16][2];
  f32x4 cs0 = {0, 0, 0, 0}, cs1 = cs0;
  const unsigned a3l0 = opq(w * 64 + (4 * q + r4) * 448 + ((q & 1) << 5) + 8 * p4), a3l1 = opq(a3l0 ^ 32);
  const unsigned a3h0 = opq(a3l0 + 4 * CELL), a3h1 = opq(a3l1 + 4 * CELL);
  auto w3rd = [&](int s, int cell) {
    frr(w3[s][0], cell < 4 ? a3l0 : a3h0, (cell & 3) * CELL, 7168); frr(w3[s][1], cell < 4 ? a3l1 : a3h1, (cell & 3) * CELL, 7168);
  };
  auto w3cs = [&](int s) { tie(w3[s][0]); tie(w3[s][1]); cs0 = mfma(fv(w3[s][0]), ones, cs0); cs1 = mfma(fv(w3[s][1]), ones, cs1); };
  w3rd(0, 0); w3rd(1, 1); w3rd(2, 4);
  w2rd(0);
  lwait<15>();
  asm volatile("s_barrier" ::: "memory");
  TS(6);
  w2rd(1);
  w3dma(5, 0); w3dma(6, 1); w3dma(7, 4);
  w3cs(0); w3cs(1); w3cs(2);
  vwait<6>();
  RT(0, chkflag(fa, sws, lane * 8, flA, tag, tag, ~0ull));
  chkflag(fa, sws, lane * 8, flA, tag, tag, ~0ull);
  v4u h0, h1, h2, h3, h4, h5, h6, h7;
  const unsigned hsrc = hb + (rq * 16 + 8 * hf) * K1;
  const unsigned hs0 = hsrc + (rot & 3) * 2048, hs1 = hsrc + ((1 + rot) & 3) * 2048, hs2 = hsrc + ((2 + rot) & 3) * 2048, hs3 = hsrc + ((3 + rot) & 3) * 2048;
  ld4_sc1<0>(h0, sws, l16, hs0); ld4_sc1<1024>(h1, sws, l16, hs0);
  ld4_sc1<0>(h2, sws, l16, hs1); ld4_sc1<1024>(h3, sws, l16, hs1);
  ld4_sc1<0>(h4, sws, l16, hs2); ld4_sc1<1024>(h5, sws, l16, hs2);
  ld4_sc1<0>(h6, sws, l16, hs3); ld4_sc1<1024>(h7, sws, l16, hs3);
  lwait<0>();
  TS(7);
  v4u x1v;
  {
    f32x4 a[4] = {};
    auto step = [&](int p, v4u& ha, v4u& hc, unsigned hs) {
      vwaitn(6 - 2 * p);
      tie(ha); tie(hc);
#pragma nounroll
      for (unsigned s = 0; s < SPIN_CAP && anyset(max4(max4(0, ha), hc)); s++) {
        reld2_sc1(ha, hc, sws, l16, hs);
        RT(1, 1);
      }
      if (p == 0) TS(8);
      if (p == 2) lwait<15>();
      if (p == 3) lwait<0>();
#pragma unroll
      for (int i = 0; i < 8; i++) tie(u[p & 1][i]);
      SB();
#pragma unroll
      for (int i = 0; i < 8; i++) a[i & 3] = mfma(fv(u[p & 1][i]), asb(i < 4 ? ha : hc), a[i & 3]);
      SB();
      if (p < 2) w2rd(p + 2);
    };
    step(0, h0, h1, hs0); step(1, h2, h3, hs1); step(2, h4, h5, hs2); step(3, h6, h7, hs3);
    *(LAS f32x4*)(aex) = a[2];
    *(LAS f32x4*)(aex + 1024) = a[3];
    bar();
    TS(9);
    f32x4 pa0, pa1;
    v2u r0, r1;
    const unsigned ar = rtile + t * 128 + (((4 * hf + (q >> 1)) ^ (t >> 1)) << 4) + 8 * (q & 1);
    lr4(pa0, aexp, 0); lr4(pa1, aexp, 1024);
    lr2(r0, ar, 0); lr2(r1, ar ^ 32, 0);
    w3rd(3, 3); w3rd(4, 5);
    w3dma(8, 8);
    lwait<8>(); tie(pa0); tie(pa1); tie(r0); tie(r1);
    a[0] += pa0; a[1] += pa1;
    x1v.x = umin(pk(a[0].x + blo(r0.x), a[0].y + bhi(r0.x)), 0xFFFFFFFEu);
    x1v.y = umin(pk(a[0].z + blo(r0.y), a[0].w + bhi(r0.y)), 0xFFFFFFFEu);
    x1v.z = umin(pk(a[1].x + blo(r1.x), a[1].y + bhi(r1.x)), 0xFFFFFFFEu);
    x1v.w = umin(pk(a[1].z + blo(r1.y), a[1].w + bhi(r1.y)), 0xFFFFFFFEu);
    st4_sc1(x1v, sws, l16, xb + myfrag);
  }
  TS(10);
  w3rd(5, 0); w3rd(6, 1); w3rd(7, 4);
  w3dma(9, 9);
  bar();
  w3dma(10, 0); w3dma(11, 1); w3dma(12, 4);
  w3cs(3); w3cs(4);
  w3dma(13, 3);
  w3cs(5); w3cs(6);
  w3dma(14, 5);
  w3cs(7);
  w3dma(15, 2);
  v4u xc0, xc1;
  ld4_sc1<0>(xc0, sws, l16, xb + w * 2048); ld4_sc1<1024>(xc1, sws, l16, xb + w * 2048);
  auto xdma = [&](unsigned c) {
#pragma unroll
    for (int i = 0; i < 16; i++) dma_sc1(sws, l16, xb + (c * 16 + i) * K1, L_X1 + (c * 16 + i) * K1);
  };
  if (w7) { xdma(1); if (lane < 4) lds_set(L_FLG + lane * 4, 0); }
  vwait2(w, 2, 18);
  bar();
  TS(11);
  {
    const unsigned b0 = opq(a3h0 + 4 * CELL), b1 = opq(a3h1 + 4 * CELL);
    frr(w3[8][0], b0, 0, 7168); frr(w3[8][1], b1, 0, 7168); frr(w3[9][0], b0, CELL, 7168); frr(w3[9][1], b1, CELL, 7168);
  }
  w3rd(10, 0); w3rd(11, 1); w3rd(12, 4); w3rd(13, 3); w3rd(14, 5); w3rd(15, 2);
  lwait<15>();
  w3cs(8); w3cs(9); w3cs(10); w3cs(11);
  lwait<0>();
  w3cs(12); w3cs(13); w3cs(14); w3cs(15);
  vwait2(w, 0, 16);
  tie(xc0); tie(xc1);
#pragma nounroll
  for (unsigned s = 0; s < SPIN_CAP && anyset(max4(max4(0, xc0), xc1)); s++) {
    reld2_sc1(xc0, xc1, sws, l16, xb + w * 2048);
    RT(2, 1);
  }
  *(LAS v4u*)(aex + (L_X1 - 2 * CELL)) = xc0;
  *(LAS v4u*)(aex + (L_X1 - 2 * CELL + 1024)) = xc1;
  const unsigned ac3 = opq(L_ST1 + w * 128 + q * 16);
  if (t == 0) { *(LAS f32x4*)(ac3) = cs0; *(LAS f32x4*)(ac3 + 64) = cs1; }
  Fr w4[7][4];
  unsigned a4[4];
  const unsigned kr = 2 * rot;
  auto w4dma = [&](int s, int i, int cell) {
    unsigned ks = s + kr; ks = ks >= 7 ? ks - 7 : ks;
    dma(sw4, vw2, (216 * w + 32 * ks + 8 * i) * K1, (cell + (i >> 1)) * CELL + w * 2 * K1 + (i & 1) * K1);
  };
  auto w4rd = [&](int s, int cell) {
#pragma unroll
    for (int j = 0; j < 4; j++) frr(w4[s][j], a4[j], (cell & 3) * CELL, CELL);
  };
#pragma unroll
  for (int j = 0; j < 4; j++) a4[j] = opq(af[j] - L_X1 - hf * 8 * K1 + w * 2 * K1);
  bar();
  TS(12);

  v4u gf[2][7];
  u64 fg;
  const unsigned vfg = lane < 7 ? (w * 8 + lane) * 8 : OOB;
  auto gld = [&](int c, int s) {
    unsigned ks = s + kr; ks = ks >= 7 ? ks - 7 : ks;
    ld4_sc1<0>(gf[c & 1][s], sws, l16, gb + (c * 56 + 7 * w + ks) * K1);
  };
  auto gneed = [&](unsigned n) {
    unsigned r = chkflag(fg, sws, vfg, flG, tag + n, tag + 4, 127);
    RT(3, r << (8 * (n - 1)));
    (void)r;
  };
  auto gpub = [&](unsigned n) { st2_sc1(tag + n, sws, vflag, flG); };
  if (w7) {
    xdma(2);
    w4dma(0, 0, 0); w4dma(0, 1, 0); w4dma(0, 2, 0); w4dma(0, 3, 0);
    w4dma(1, 0, 2); w4dma(1, 1, 2); w4dma(1, 2, 2); w4dma(1, 3, 2);
    xdma(3);
    const unsigned ax = opq(L_X1 + l16);
#pragma nounroll
    for (unsigned c = 0; c < 4; c++) {
      v4u f[16];
      const unsigned axc = ax + c * CELL;
      if (c == 1) vwait<40>(); else if (c == 2) vwait<24>(); else if (c == 3) vwait<0>();
#pragma nounroll
      for (unsigned s = 0; s < SPIN_CAP; s++) {
#pragma unroll
        for (int i = 0; i < 16; i++) lr4(f[i], axc, i * 1024);
        lwait<0>();
        unsigned mx = 0;
#pragma unroll
        for (int i = 0; i < 16; i++) { tie(f[i]); mx = max4(mx, f[i]); }
        if (!anyset(mx)) break;
        vwait<0>(); xdma(c); vwait<0>();
        RT(2, 256);
      }
      lds_set(L_FLG, c);
      f32x4 rs = {0, 0, 0, 0}, sq = rs;
#pragma unroll
      for (int i = 0; i < 16; i++) { rs = mfma(ones, asb(f[i]), rs); sq = mfma(asb(f[i]), asb(f[i]), sq); }
      float s2 = __builtin_bit_cast(float, __builtin_amdgcn_ds_bpermute((20 * (t >> 2) + (t & 3)) * 4, __builtin_bit_cast(int, diag(sq, t))));
      f32x4 st = mkstats(rs.x, s2);
      if (lane < 16) *(LAS f32x4*)(L_ST3 + (c * 16 + lane) * 16) = st;
      lwait<0>();
      lds_set(L_FLG + 4, c + 1);
    }
    TS(13);
    w4rd(0, 0); w4rd(1, 2);
    lwait<0>();
#pragma unroll
    for (int i = 0; i < 4; i++) w4dma(2, i, 0);
#pragma unroll
    for (int i = 0; i < 4; i++) w4dma(3, i, 2);
    vwait<0>();
    w4rd(2, 0); w4rd(3, 2);
    lwait<0>();
#pragma unroll
    for (int i = 0; i < 4; i++) w4dma(4, i, 0);
#pragma unroll
    for (int i = 0; i < 4; i++) w4dma(5, i, 2);
    lds_wait_gt(L_FLG + 8, 6);
#pragma unroll
    for (int i = 0; i < 4; i++) w4dma(6, i, 6);
    u64 fl = 0;
    TS(14);
    unsigned r = chkflag(fl, sws, lane * 8, flG, tag + 1, tag + 4, 0x7f7f7f7f7f7f7f7full);
    RT(3, r);
    lds_set(L_FLG + 12, 1);
#pragma unroll
    for (int s = 0; s < 7; s++) gld(0, s);
    w4rd(4, 0); w4rd(5, 2);
#pragma unroll
    for (int j = 0; j < 4; j++) a4[j] += 4 * CELL;
    w4rd(6, 6);
    TS(15);
    vwait<0>();
    r = chkflag(fl, sws, lane * 8, flG, tag + 2, tag + 4, 0x7f7f7f7f7f7f7f7full);
    RT(3, r << 8);
    (void)r;
    lds_set(L_FLG + 12, 2);
    gld(1, 0); gld(1, 1);
    ld2_sc1(fg, sws, vfg, flG);
#pragma unroll
    for (int s = 2; s < 7; s++) gld(1, s);
    TS(16);
    lwait<0>();
  } else {
    const unsigned ast = opq(L_ST3 + t * 16);
#pragma unroll
    for (int c = 0; c < 4; c++) {
      if (c > 0) lds_wait_gt(L_FLG, c - 1);
      v4u xq[2][4];
      auto xrd = [&](int g) {
        const unsigned xa = opq(l16) + (L_X1 + ((g + rot) & 3) * 4096);
#pragma unroll
        for (int i = 0; i < 4; i++) lr4(xq[g & 1][i], xa, c * CELL + i * 1024);
      };
      xrd(0); xrd(1);
      f32x4 a0 = {0, 0, 0, 0}, a1 = a0;
#pragma unroll
      for (int g = 0; g < 4; g++) {
        if (c == 3 && g == 2) lds_wait_gt(L_FLG + 12, 0);
        else lwaitn(g == 3 ? 0 : 4);
#pragma unroll
        for (int i = 0; i < 4; i++) tie(xq[g & 1][i]);
#pragma unroll
        for (int i = 0; i < 4; i++) {
          SB();
          const int s = 4 * g + i;
          a0 = mfma(fv(w3[s][0]), asb(xq[g & 1][i]), a0); a1 = mfma(fv(w3[s][1]), asb(xq[g & 1][i]), a1);
          SB();
          if (c == 0 && g < 3) w4dma(g, i, g == 0 ? 4 : g == 1 ? 2 : 0);
          if (c == 2 && g < 3) w4dma(3 + g, i, g == 0 ? 4 : g == 1 ? 2 : 0);
          if (c == 2 && g == 3) w4dma(6, i, 6);
          if (c == 3 && s >= 8 && s < 15) gld(0, s - 8);
        }
        if (g < 2) xrd(g + 2);
        if (c == 2 && g == 2) lds_wait_gt(L_FLG + 8, 6);
      }
      f32x4 st;
      if (c == 1) {
        if (lane == 0) __hip_atomic_fetch_add((LAS unsigned*)(L_FLG + 8), 1u, __ATOMIC_RELAXED, __HIP_MEMORY_SCOPE_WORKGROUP);
      }
      if (c == 1) { vwait<0>(); gpub(1); }
      if (c == 2) { vwait<16>(); gpub(2); }
      if (c == 3) { vwait<7>(); gpub(3); }
      lds_wait_gt(L_FLG + 4, c);
      lr4(st, ast, c * 256);
      lr4(cs0, ac3, 0); lr4(cs1, ac3, 64);
      if (c == 1) {
#pragma unroll
        for (int j = 0; j < 4; j++) frr(w4[0][j], opq(a4[j]) + 4 * CELL, 0, CELL);
        lwait<8>();
        w4rd(1, 2); w4rd(2, 0);
      } else if (c == 3) {
#pragma unroll
        for (int j = 0; j < 4; j++) { const unsigned ah = opq(a4[j]) + 4 * CELL; frr(w4[3][j], ah, 0, CELL); frr(w4[6][j], ah, 2 * CELL, CELL); if (j == 1) lwait<8>(); }
        w4rd(4, 2); w4rd(5, 0);
      } else lwait<0>();
      tie(st); tie(cs0); tie(cs1);
      v4u gv = gelu_frag(a0, a1, cs0, cs1, st);
      st4_sc1(gv, sws, l16, gb + (c * 56 + 7 * m + w) * K1);
      TS(13 + c);
    }
    lds_wait_gt(L_FLG + 12, 1);
    gld(1, 0); gld(1, 1);
    ld2_sc1(fg, sws, vfg, flG);
#pragma unroll
    for (int s = 2; s < 7; s++) gld(1, s);
  }
#pragma unroll
  for (int s = 0; s < 7; s++)
#pragma unroll
    for (int j = 0; j < 4; j++) tie(w4[s][j]);
  TS(17);
  {
    const unsigned apw = (t >> 3) * CELL + w * 2 * K1 + (t & 7) * 256 + ((8 * hf) ^ q ^ t) * 16;
    unsigned pw[4];
#pragma unroll
    for (int j = 0; j < 4; j++) pw[j] = opq(apw ^ (64 * j));
    const unsigned rr = 2 * w + (lane >> 5);
    const unsigned apr = opq((rr >> 3) * CELL + (rr & 7) * 256 + ((((lane & 31) >> 1) ^ rr) << 4) + 8 * (lane & 1));
    const unsigned vo = (row0 + rr) * 1024 + 128 * m + 4 * (lane & 31);
    f32x2 pr[8];
    auto fin = [&](int c) {
      lwait<0>();
#pragma unroll
      for (int p = 0; p < 8; p++) tie(pr[p]);
      f32x2 o = ((pr[0] + pr[1]) + (pr[2] + pr[3])) + ((pr[4] + pr[5]) + (pr[6] + pr[7]));
      st1(pk(o.x, o.y), so, vo, c * CELL);
    };
#pragma unroll
    for (int c = 0; c < 4; c++) {
      const unsigned mk = rq == c ? ~0u : 0u;
      f32x4 a[4] = {};
      a[0] = (f32x4){blo(x1v.x & mk), bhi(x1v.x & mk), blo(x1v.y & mk), bhi(x1v.y & mk)};
      a[1] = (f32x4){blo(x1v.z & mk), bhi(x1v.z & mk), blo(x1v.w & mk), bhi(x1v.w & mk)};
      if (c == 1) { vwait<6>(); gneed(3); }
      if (c == 2) { vwait<6>(); gneed(4); }
#pragma unroll
      for (int s = 0; s < 7; s++) {
        if (c == 0 && s < 4) vwaitn(16 - s);
        if (c == 1 && s >= 2) vwait<8>();
        if (c == 2 && s >= 2) vwait<7>();
        if (c == 3) vwaitn(7 - s);
        tie(gf[c & 1][s]);
        SB();
#pragma unroll
        for (int j = 0; j < 4; j++) a[j] = mfma(fv(w4[s][j]), asb(gf[c & 1][s]), a[j]);
        SB();
        if (c == 0 && s == 3) { vwait<8>(); st2_sc1(w7 ? tag : tag + 4, sws, w7 ? ((m == 0 && lane == 0) ? 0 : OOB) : vflag, w7 ? WS_EP + group * 128 : flG); }
        if (c == 1 || c == 2) {
          gld(c + 1, s);
          if (c == 1 && s == 1) ld2_sc1(fg, sws, vfg, flG);
        }
      }
      if (c > 0) fin(c - 1);
#pragma unroll
      for (int j = 0; j < 4; j++) *(LAS f32x4*)(pw[j] + (c & 1) * 2 * CELL) = a[j];
      bar();
#pragma unroll
      for (int p = 0; p < 8; p++) lr2(pr[p], apr, (c & 1) * 2 * CELL + p * 2 * K1);
      if (c == 0) TS(18);
    }
    fin(3);
  }
}

extern "C" void kernel_launch(void* const* d_in, const int* in_sizes, int n_in, void* d_out, int out_size, void* d_ws, size_t ws_size,
                              hipStream_t stream) {
  (void)hipFuncSetAttribute((const void*)mixer, hipFuncAttributeMaxDynamicSharedMemorySize, L_TOTAL);
  mixer<<<256, 512, L_TOTAL, stream>>>(d_in[0], d_in[1], d_in[2], d_in[3], d_in[4], d_out, d_ws);
}
```

```cpp
#include <hip/hip_runtime.h>
#define LAS __attribute__((address_space(3)))
typedef __bf16 bf16x8 __attribute__((ext_vector_type(8)));
typedef __bf16 bf16x2 __attribute__((ext_vector_type(2)));
typedef short s16x4 __attribute__((ext_vector_type(4)));
typedef short s16x8 __attribute__((ext_vector_type(8)));
typedef float f32x4 __attribute__((ext_vector_type(4)));
typedef float f32x2 __attribute__((ext_vector_type(2)));
typedef unsigned v4u __attribute__((ext_vector_type(4)));
typedef unsigned v2u __attribute__((ext_vector_type(2)));
typedef __amdgpu_buffer_rsrc_t srd_t;
typedef unsigned long long u64;

constexpr unsigned K1 = 1024u, CELL = 16 * K1;
constexpr unsigned WS_EP = 0, WS_FL = 8192, WS_D = 65536, WGB = 48 * K1, WS_END = WS_D + 256u * WGB;
constexpr unsigned L_W2 = 64 * K1, L_R1 = 128 * K1, L_ST1 = 16 * K1, L_CS = 18 * K1, L_ST3 = 156 * K1, L_FLG = 157 * K1, L_TOTAL = 160 * K1;
#define NEWLAY 1
#define XBAR 1
#define OPT 0
#define MAP8 0
#define LATE3 0
#define NOCLR 0
constexpr unsigned OOB = 0x80000000u;
#define SPINC 200000u
constexpr unsigned SPIN_CAP = SPINC;

#define FI __device__ __forceinline__
#define SB() __builtin_amdgcn_sched_barrier(0)
FI void dma(srd_t s, unsigned voff, unsigned soff, unsigned lds) {
  asm volatile("s_mov_b32 m0, %0\n\ts_nop 0\n\tbuffer_load_dwordx4 %1, %2, %3 offen lds" ::"s"(lds), "v"(voff), "s"(s), "s"(soff) : "memory");
}
FI void dma_sc1(srd_t s, unsigned voff, unsigned soff, unsigned lds) {
  asm volatile("s_mov_b32 m0, %0\n\ts_nop 0\n\tbuffer_load_dwordx4 %1, %2, %3 offen sc1 lds" ::"s"(lds), "v"(voff), "s"(s), "s"(soff) : "memory");
}
template <int OFF> FI void ld4_sc1(v4u& d, srd_t s, unsigned voff, unsigned soff) {
  asm volatile("buffer_load_dwordx4 %0, %1, %2, %3 offen offset:%4 sc1" : "=v"(d) : "v"(voff), "s"(s), "s"(soff), "n"(OFF) : "memory");
}
FI void reld2_sc1(v4u& d0, v4u& d1, srd_t s, unsigned voff, unsigned soff) {
  asm volatile("buffer_load_dwordx4 %0, %2, %3, %4 offen sc1\n\tbuffer_load_dwordx4 %1, %2, %3, %4 offen offset:1024 sc1\n\ts_waitcnt vmcnt(0)"
               : "+v"(d0), "+v"(d1) : "v"(voff), "s"(s), "s"(soff) : "memory");
}
template <int OFF> FI void ld4(v4u& d, srd_t s, unsigned voff, unsigned soff) {
  asm volatile("buffer_load_dwordx4 %0, %1, %2, %3 offen offset:%4" : "=v"(d) : "v"(voff), "s"(s), "s"(soff), "n"(OFF) : "memory");
}
template <int OFF> FI void ld4o(v4u& d, srd_t s, unsigned voff, unsigned soff) { if (OPT & 1) ld4<OFF>(d, s, voff, soff); else ld4_sc1<OFF>(d, s, voff, soff); }
FI void ld2_sc1(u64& d, srd_t s, unsigned voff, unsigned soff) {
  asm volatile("buffer_load_dwordx2 %0, %1, %2, %3 offen sc1" : "=v"(d) : "v"(voff), "s"(s), "s"(soff) : "memory");
}
FI void st4_sc1(v4u d, srd_t s, unsigned voff, unsigned soff) {
  asm volatile("buffer_store_dwordx4 %0, %1, %2, %3 offen sc1\n\ts_nop 1" ::"v"(d), "v"(voff), "s"(s), "s"(soff) : "memory");
}
FI void st1(unsigned d, srd_t s, unsigned voff, unsigned soff) {
  asm volatile("buffer_store_dword %0, %1, %2, %3 offen" ::"v"(d), "v"(voff), "s"(s), "s"(soff) : "memory");
}
FI void st1_sc1(unsigned d, srd_t s, unsigned voff, unsigned soff) {
  asm volatile("buffer_store_dword %0, %1, %2, %3 offen sc1" ::"v"(d), "v"(voff), "s"(s), "s"(soff) : "memory");
}
FI void lw4(unsigned a, f32x4 d, int off) { asm volatile("ds_write_b128 %0, %1 offset:%2" ::"v"(a), "v"(d), "i"(off) : "memory"); }
FI void st2_sc1(u64 d, srd_t s, unsigned voff, unsigned soff) {
  asm volatile("buffer_store_dwordx2 %0, %1, %2, %3 offen sc1" ::"v"(d), "v"(voff), "s"(s), "s"(soff) : "memory");
}
template <int N> FI void vwait() { asm volatile("s_waitcnt vmcnt(%0)" ::"n"(N) : "memory"); }
template <int N> FI void lwait() { asm volatile("s_waitcnt lgkmcnt(%0)" ::"n"(N) : "memory"); }
FI void bar() { asm volatile("s_waitcnt lgkmcnt(0)\n\ts_barrier" ::: "memory"); }
template <class T> FI void tie(T& a) { asm volatile("" : "+v"(a)); }
struct Fr { s16x4 lo, hi; };
FI void tie(Fr& f) { asm volatile("" : "+v"(f.lo), "+v"(f.hi)); }
FI void trr(s16x4& d, unsigned a, int off) { asm volatile("ds_read_b64_tr_b16 %0, %1 offset:%2" : "=v"(d) : "v"(a), "i"(off) : "memory"); }
FI void frr(Fr& f, unsigned a, int off, int sec) { trr(f.lo, a, off); trr(f.hi, a, off + sec); }
template <class T> FI void lr4(T& d, unsigned a, int off) { asm volatile("ds_read_b128 %0, %1 offset:%2" : "=v"(d) : "v"(a), "i"(off) : "memory"); }
template <class T> FI void lr2(T& d, unsigned a, int off) { asm volatile("ds_read_b64 %0, %1 offset:%2" : "=v"(d) : "v"(a), "i"(off) : "memory"); }
FI void vwaitn(int n) { asm volatile("s_waitcnt vmcnt(%0)" ::"i"(n) : "memory"); }
FI void lwaitn(int n) { asm volatile("s_waitcnt lgkmcnt(%0)" ::"i"(n) : "memory"); }
FI bf16x8 fv(const Fr& f) { return __builtin_bit_cast(bf16x8, __builtin_shufflevector(f.lo, f.hi, 0, 1, 2, 3, 4, 5, 6, 7)); }
FI f32x4 mfma(bf16x8 a, bf16x8 b, f32x4 c) { return __builtin_amdgcn_mfma_f32_16x16x32_bf16(a, b, c, 0, 0, 0); }
FI bf16x8 asb(v4u v) { return __builtin_bit_cast(bf16x8, v); }
FI unsigned pk(float a, float b) {
  f32x2 f = {a, b};
  return __builtin_bit_cast(unsigned, __builtin_convertvector(f, bf16x2));
}
FI float asf(unsigned u) { return __builtin_bit_cast(float, u); }
FI unsigned asu(float u) { return __builtin_bit_cast(unsigned, u); }
FI bf16x2 asb2(unsigned u) { return __builtin_bit_cast(bf16x2, u); }
FI unsigned umin(unsigned a, unsigned b) { return a < b ? a : b; }
FI unsigned umax(unsigned a, unsigned b) { return a > b ? a : b; }
FI unsigned max4(unsigned m, v4u v) { return umax(umax(umax(m, v.x), v.y), umax(v.z, v.w)); }
FI bool anyset(unsigned mx) { return __builtin_amdgcn_ballot_w64(mx == ~0u) != 0; }
FI float blo(unsigned u) { return __builtin_bit_cast(float, u << 16); }
FI float bhi(unsigned u) { return __builtin_bit_cast(float, u & 0xffff0000u); }
FI unsigned opq(unsigned a) { asm volatile("" : "+v"(a)); return a; }
FI float gelu_n(float a, float k1, float k2, float irs) {
  float u = __builtin_fmaf(a * a, k2, k1);
  float e = __builtin_amdgcn_exp2f(a * u);
  return a * __builtin_amdgcn_rcpf(__builtin_fmaf(e, irs, irs));
}
FI v4u gelu_frag(f32x4 a0, f32x4 a1, f32x4 c0, f32x4 c1, f32x4 st) {
  float o[8];
#pragma unroll
  for (int j = 0; j < 4; j++) {
    o[j] = gelu_n(__builtin_fmaf(-st.x, c0[j], a0[j]), st.y, st.z, st.w);
    o[4 + j] = gelu_n(__builtin_fmaf(-st.x, c1[j], a1[j]), st.y, st.z, st.w);
  }
  v4u r = {pk(o[0], o[1]), pk(o[2], o[3]), pk(o[4], o[5]), pk(o[6], o[7])};
  return r;
}
FI f32x4 mkstats(float s1, float s2) {
  float mean = s1 * (1.f / 512.f);
  float var = __builtin_fmaf(s2, 1.f / 512.f, -mean * mean);
  var = (var > 0.f ? var : 0.f) + 1e-5f;
  float rstd = __builtin_amdgcn_rsqf(var);
  f32x4 r = {mean, -2.302208198f * rstd, -0.1029432397f * rstd * rstd * rstd, var * rstd};
  return r;
}
FI float diag(f32x4 sq, unsigned t) { return (t & 2) ? ((t & 1) ? sq.w : sq.z) : ((t & 1) ? sq.y : sq.x); }
FI void lds_wait_gt(unsigned addr, unsigned v) {
  unsigned x, n;
  asm volatile("s_mov_b32 %1, %4\n1:\n\tds_read_b32 %0, %2\n\ts_waitcnt lgkmcnt(0)\n\tv_cmp_lt_u32 vcc, %3, %0\n\ts_cbranch_vccnz 2f\n\t"
               "s_sub_u32 %1, %1, 1\n\ts_cbranch_scc0 1b\n2:"
               : "=&v"(x), "=&s"(n) : "v"(addr), "i"(v), "i"(SPIN_CAP * 16u) : "memory", "vcc", "scc");
}
FI unsigned chkflag(u64& f, srd_t s, unsigned voff, unsigned soff, u64 lo, u64 hi, u64 mask) {
  unsigned n; u64 tmp;
  asm volatile("s_mov_b32 %1, %9\n1:\n\tv_cmp_gt_u64 vcc, %6, %0\n\tv_cmp_lt_u64 %2, %7, %0\n\ts_or_b64 vcc, vcc, %2\n\t"
               "s_and_b64 vcc, vcc, %8\n\ts_cbranch_scc0 2f\n\tbuffer_load_dwordx2 %0, %3, %4, %5 offen sc1\n\ts_waitcnt vmcnt(0)\n\t"
               "s_sub_u32 %1, %1, 1\n\ts_cbranch_scc0 1b\n2:"
               : "+v"(f), "=&s"(n), "=&s"(tmp) : "v"(voff), "s"(s), "s"(soff), "s"(lo), "s"(hi), "s"(mask), "i"(SPIN_CAP) : "memory", "vcc", "scc");
  return SPIN_CAP - n;
}
FI void rel4_sc1(v4u& d, srd_t s, unsigned voff, unsigned soff) {
  asm volatile("buffer_load_dwordx4 %0, %1, %2, %3 offen sc1" : "+v"(d) : "v"(voff), "s"(s), "s"(soff) : "memory");
}
FI v4u nosent(v4u v) { v4u r = {umin(v.x, 0xFFFFFFFEu), umin(v.y, 0xFFFFFFFEu), umin(v.z, 0xFFFFFFFEu), umin(v.w, 0xFFFFFFFEu)}; return r; }
FI float ror8(float v) { return __builtin_bit_cast(float, __builtin_amdgcn_update_dpp(0, __builtin_bit_cast(int, v), 0x128, 0xf, 0xf, false)); }
FI void lds_set(unsigned addr, unsigned v) { asm volatile("ds_write_b32 %0, %1" ::"v"(addr), "v"(v) : "memory"); }
#define TS(i)
#define RT(i, n)
FI void vwait2(unsigned w, int a, int b) {
  asm volatile("s_waitcnt vmcnt(%2)\n\ts_cmp_eq_u32 %0, 7\n\ts_cbranch_scc1 1f\n\ts_waitcnt vmcnt(%1)\n1:" ::"s"(w), "i"(a), "i"(b) : "memory", "scc");
}

__global__ __launch_bounds__(512, 2) void mixer(const void* __restrict__ px, const void* __restrict__ pw1, const void* __restrict__ pw2,
                                                const void* __restrict__ pw3, const void* __restrict__ pw4, void* pout, void* pws) {
  const unsigned lane = threadIdx.x & 63;
  const unsigned w = __builtin_amdgcn_readfirstlane(threadIdx.x >> 6);
  const bool w7 = w == 7;
  const unsigned b = blockIdx.x, m = 4 * (b & 1) + ((b >> 3) & 3), gi = b >> 5, group = ((b & 7) >> 1) * 8 + gi, row0 = group * 64;
  const unsigned rq = w >> 1, hf = w & 1;
  const unsigned t = lane & 15, q = lane >> 4, r4 = (lane >> 2) & 3, p4 = lane & 3;
  const srd_t sx = __builtin_amdgcn_make_buffer_rsrc((void*)px, 0, 2048 * 1024, 0x00020000);
  const srd_t sw1 = __builtin_amdgcn_make_buffer_rsrc((void*)pw1, 0, 512 * 1024, 0x00020000);
  const srd_t sw2 = __builtin_amdgcn_make_buffer_rsrc((void*)pw2, 0, 512 * 1024, 0x00020000);
  const srd_t sw3 = __builtin_amdgcn_make_buffer_rsrc((void*)pw3, 0, 512 * 3584, 0x00020000);
  const srd_t sw4 = __builtin_amdgcn_make_buffer_rsrc((void*)pw4, 0, 1792 * 1024, 0x00020000);
  const srd_t so = __builtin_amdgcn_make_buffer_rsrc(pout, 0, 2048 * 1024, 0x00020000);
  const srd_t sws = __builtin_amdgcn_make_buffer_rsrc(pws, 0, WS_END, 0x00020000);
  asm volatile("" ::"s"(pw4), "s"(pout));
  u64 epoch;
  asm volatile("s_load_dwordx2 %0, %1, 0" : "=s"(epoch) : "s"((const char*)pws + WS_EP + group * 128) : "memory");
  const unsigned l16 = lane * 16;
  const unsigned gbase = WS_D + group * 8 * WGB, mine = gbase + m * WGB;

  const unsigned rho = lane >> 3;
  const unsigned wsw = 128 * m + (((((lane & 7) >> 1) ^ q) << 1) | (lane & 1)) * 16;
  const unsigned vw2 = (8 * w + rho) * 1024 + wsw;
  const unsigned vw1 = (32 * (w >> 2) + 16 * (w & 1) + 4 * ((w >> 1) & 1) + 8 * (rho >> 2) + (rho & 3)) * 1024 + wsw;
  unsigned vxa, vxb;
  {
    const int cg = m >> 1;
    const int dl = (cg & 1) ? (cg >> 1) + 1 : -((cg >> 1) + 1);
    const int ra = (int)(row0 + 16 * rq + (t & 7)) + dl, rb = ra + 8;
    const unsigned cb = hf * 512 + 16 * q + 8 * (t & 8);
    vxa = (ra >= 0 && ra < 2048) ? ra * 1024 + cb : OOB;
    vxb = (rb >= 0 && rb < 2048) ? rb * 1024 + (cb ^ 64) : OOB;
  }
  v4u xa[4], xb[4], res;
  auto p1ld = [&](int p) {
    const unsigned pp = (p + gi + m) & 3;
    dma(sw1, vw1, pp * 64 * K1, p * CELL + w * K1);
    ld4<0>(xa[p], sx, vxa, pp * 128);
    dma(sw1, vw1, pp * 64 * K1 + 256 * K1, p * CELL + 8 * K1 + w * K1);
    ld4<0>(xb[p], sx, vxb, pp * 128);
  };
  p1ld(0);
  p1ld(1);
  const v4u SENT = {~0u, ~0u, ~0u, ~0u};
  asm volatile("s_barrier" ::: "memory");
  st4_sc1(SENT, sws, NOCLR ? OOB : l16, mine + w * K1);
  st4_sc1(SENT, sws, NOCLR ? OOB : l16, mine + (8 + w) * K1);
  st4_sc1(SENT, sws, lane < 8 ? l16 : OOB, mine + 44 * K1 + w * 128);
  unsigned pf = 0;
  asm volatile("s_load_dword %0, %1, 0\n\ts_load_dword %0, %1, 64\n\ts_load_dword %0, %1, 128\n\ts_load_dword %0, %1, 192\n\t"
               "s_load_dword %0, %1, 256\n\ts_load_dword %0, %1, 320\n\ts_load_dword %0, %1, 384\n\ts_load_dword %0, %1, 448\n\t"
               "s_load_dword %0, %1, 512\n\ts_load_dword %0, %1, 576\n\ts_load_dword %0, %1, 640\n\ts_load_dword %0, %1, 704\n\t"
               "s_load_dword %0, %1, 768\n\ts_load_dword %0, %1, 832"
               : "+s"(pf) : "s"((const char*)pw3 + (b * 8 + w) * 896) : "memory");
  p1ld(2);
  p1ld(3);
  ld4<0>(res, sx, (row0 + 16 * rq + t) * 1024 + 128 * m + 64 * hf + 16 * (((q & 1) << 1) | (q >> 1)), 0);
  auto w2dma = [&](int p, int h2) { dma(sw2, vw2, (((p + gi) & 3) * 64 + h2 * 256) * K1, L_W2 + p * CELL + h2 * 8 * K1 + w * K1); };
  const unsigned r1 = L_R1 + w * 4 * K1, r2 = L_W2 + w * 8 * K1;
  const unsigned vw3 = (lane >> 2) * 3584 + 448 * m + 64 * w + ((p4 ^ ((q & 1) << 1)) << 4);
  auto w3dma = [&](int s) {
    const unsigned so3 = ((s + 2 * gi) & 15) * 32 * 3584, sl = s % 6, ld = sl < 2 ? r1 + sl * 2 * K1 : r2 + (sl - 2) * 2 * K1;
    dma(sw3, vw3, so3, ld);
    dma(sw3, vw3, so3 + 16 * 3584, ld + K1);
  };
  const unsigned kr = gi == 7 ? 0 : gi;
  auto w4dma = [&](int s, int pr) {
    unsigned ks = s + kr; ks = ks >= 7 ? ks - 7 : ks;
#pragma unroll
    for (int i = 0; i < 4; i++) dma(sw4, vw2, (216 * w + 32 * ks + 8 * i) * K1, (pr == 0 ? r1 : r2 + (pr - 1) * 4 * K1) + i * K1);
  };
  u64 tag = 0;
  const unsigned flA = WS_FL + group * 128;
  const unsigned g2 = ((q & 1) << 1) | (r4 >> 1);
  unsigned af[4];
#pragma unroll
  for (int j = 0; j < 4; j++) af[j] = opq((4 * q + r4) * 128 + 8 * p4 + (((j ^ (2 * hf)) ^ g2) << 5) + hf * 8 * K1);
  TS(1);

  v4u onesv = {0x3F803F80u, 0x3F803F80u, 0x3F803F80u, 0x3F803F80u};
  tie(onesv);
  const bf16x8 ones = asb(onesv);
  const unsigned afc = opq((4 * q + r4) * 128 + 8 * p4 + ((rq ^ g2) << 5) + hf * 8 * K1);
  u64 fa = 0;
  unsigned touch = 0;
  {
    f32x4 a[4] = {}, ap[4] = {}, rs0 = {0, 0, 0, 0}, sq0 = rs0, rs1 = rs0, sq1 = rs0, cc = rs0;
#pragma unroll
    for (int hp = 0; hp < 2; hp++) {
      if (hp == 0) vwait<12>(); else vwait<5>();
      bar();
      if (hp == 0) {
        asm volatile("" : "+s"(pf), "+s"(epoch));
        tag = epoch * 6364136223846793005ull + 1442695040888963407ull;
        TS(2);
      } else TS(3);
      Fr f[16], fc[4];
#pragma unroll
      for (int i = 0; i < 16; i++) {
        frr(f[i], af[i & 3], (2 * hp + (i >> 3)) * CELL + ((i >> 2) & 1) * 4096, 2048);
        if ((i & 3) == 3) frr(fc[i >> 2], afc, (2 * hp + (i >> 3)) * CELL + ((i >> 2) & 1) * 4096, 2048);
      }
      if (hp == 1 && w7) st2_sc1(tag, sws, lane == 0 ? m * 8 : OOB, flA);
      v4u x0[2], x1[2];
#pragma unroll
      for (int p = 0; p < 2; p++) {
        tie(xa[2 * hp + p]); tie(xb[2 * hp + p]);
        const bool lo = t < 8;
        x0[p] = lo ? xa[2 * hp + p] : xb[2 * hp + p];
        x1[p] = lo ? xb[2 * hp + p] : xa[2 * hp + p];
        rs0 = mfma(ones, asb(x0[p]), rs0); sq0 = mfma(asb(x0[p]), asb(x0[p]), sq0);
        rs1 = mfma(ones, asb(x1[p]), rs1); sq1 = mfma(asb(x1[p]), asb(x1[p]), sq1);
        tie(rs0); tie(sq0); tie(rs1); tie(sq1);
        SB();
        w2dma(2 * hp, p);
      }
#pragma unroll
      for (int g = 0; g < 4; g++) {
        lwaitn(g == 0 ? 15 : g == 1 ? 15 : g == 2 ? 10 : 0);
#pragma unroll
        for (int j = 0; j < 4; j++) tie(f[4 * g + j]);
        tie(fc[g]);
        SB();
#pragma unroll
        for (int j = 0; j < 4; j++) {
          if (g & 1) ap[j] = mfma(fv(f[4 * g + j]), asb(x1[g >> 1]), ap[j]);
          else a[j] = mfma(fv(f[4 * g + j]), asb(x0[g >> 1]), a[j]);
        }
        cc = mfma(fv(fc[g]), ones, cc);
        SB();
        if (g < 2) w2dma(2 * hp + 1, g);
        else if (hp == 1 && !w7 && !LATE3) w3dma(g - 2);
      }
    }
#pragma unroll
    for (int j = 3; j >= 0; j--) {
      a[j].x += ror8(ap[j].x); a[j].y += ror8(ap[j].y); a[j].z += ror8(ap[j].z); a[j].w += ror8(ap[j].w);
      if (j >= 2) *(LAS f32x4*)(w * 2 * K1 + (j - 2) * K1 + l16) = a[j];
    }
    if (q == (t >> 2)) {
      f32x2 s0 = {rs0.x, diag(sq0, t)}, s1 = {rs1.x, diag(sq1, t)};
      *(LAS f32x2*)(L_ST1 + w * 256 + t * 8) = s0;
      *(LAS f32x2*)(L_ST1 + w * 256 + 128 + (t ^ 8) * 8) = s1;
    }
    if (t == 0) *(LAS f32x4*)(L_CS + hf * 256 + rq * 64 + q * 16) = cc;
    bar();
    TS(4);
    f32x4 pa0, pa1, c00, c01, c10, c11;
    f32x2 s0, s1, s2, s3;
    {
      const unsigned as = L_ST1 + (w & 6) * 256 + t * 8, ac = L_CS + hf * 128 + q * 16, ae = (w ^ 1) * 2 * K1 + l16;
      lr2(s0, as, 0); lr2(s1, as, 128); lr2(s2, as, 256); lr2(s3, as, 384);
      lr4(pa0, ae, 0); lr4(pa1, ae, 1024);
      lr4(c00, ac, 0); lr4(c01, ac, 64); lr4(c10, ac, 256); lr4(c11, ac, 320);
    }
    if (w7) ld2_sc1(fa, sws, (lane & 7) * 8, flA);
    lwait<6>(); tie(s0); tie(s1); tie(s2); tie(s3);
    f32x4 st = mkstats((s0.x + s1.x) + (s2.x + s3.x), (s0.y + s1.y) + (s2.y + s3.y));
    lwait<0>(); tie(pa0); tie(pa1); tie(c00); tie(c01); tie(c10); tie(c11);
    st4_sc1(nosent(gelu_frag(a[0] + pa0, a[1] + pa1, c00 + c10, c01 + c11, st)), sws, l16, mine + w * K1);
  }
  TS(5);
  if (w7) {
    vwait<1>();
    unsigned r = chkflag(fa, sws, (lane & 7) * 8, flA, tag, tag, ~0ull);
    RT(0, r); (void)r;
    if (lane == 0) lds_set(L_FLG, 0);
  } else {
    vwait<LATE3 ? 1 : 5>();
  }
  bar();
  TS(6);
  v4u h0, h1, h2, h3, h4, h5, h6, h7;
  const unsigned hsrc = gbase + 4 * hf * WGB + 2 * rq * K1;
  const unsigned hs0 = hsrc + (gi & 3) * WGB, hs1 = hsrc + ((1 + gi) & 3) * WGB, hs2 = hsrc + ((2 + gi) & 3) * WGB, hs3 = hsrc + ((3 + gi) & 3) * WGB;
  v4u x1v;
  const unsigned aim = opq(w * 8 * K1 + l16);
  {
    Fr u[32];
#pragma unroll
    for (int j = 0; j < 4; j++) af[j] = opq(af[j] + L_W2);
#pragma unroll
    for (int i = 0; i < 32; i++) {
      frr(u[i], af[i & 3], (i >> 3) * CELL + ((i >> 2) & 1) * 4096, 2048);
      if (i == 3) ld4o<0>(h0, sws, l16, hs0);
      if (i == 7) { ld4o<1024>(h1, sws, l16, hs0); }
      if (i == 11) ld4o<0>(h2, sws, l16, hs1);
      if (i == 15) { ld4o<1024>(h3, sws, l16, hs1); }
      if (i == 19) ld4o<0>(h4, sws, l16, hs2);
      if (i == 23) { ld4o<1024>(h5, sws, l16, hs2); }
      if (i == 27) ld4o<0>(h6, sws, l16, hs3);
      if (i == 31) { ld4o<1024>(h7, sws, l16, hs3); }
    }
    bar();
    TS(7);
    if (w7) {
      w4dma(0, 1); w4dma(1, 2);
      asm volatile("buffer_load_dword %0, %1, %2, 0 offen" : "+v"(touch) : "v"(lane < 56 ? (b * 56 + lane) * 128 : OOB), "s"(sw4) : "memory");
    }
    else { if (LATE3) { w3dma(0); w3dma(1); } w3dma(2); w3dma(3); w3dma(4); w3dma(5); }
    f32x4 a[4] = {};
    auto step = [&](int p, v4u& ha, v4u& hc, unsigned hs) {
      if (LATE3) { if (w7) vwaitn(15 - 2 * p); else vwaitn(18 - 2 * p); } else vwait2(w, 14 - 2 * p, 15 - 2 * p);
      tie(ha); tie(hc);
#pragma nounroll
      for (unsigned s = 0; s < SPIN_CAP && anyset(max4(max4(0, ha), hc)); s++) {
        reld2_sc1(ha, hc, sws, l16, hs);
        RT(1, 1);
      }
      if (p == 0) { tie(res); TS(8); }
#pragma unroll
      for (int i = 0; i < 8; i++) tie(u[8 * p + i]);
      SB();
#pragma unroll
      for (int i = 0; i < 8; i++) a[(i + 2) & 3] = mfma(fv(u[8 * p + ((i + 2) & 3) + (i & 4)]), asb(i < 4 ? ha : hc), a[(i + 2) & 3]);
      SB();
    };
    step(0, h0, h1, hs0); step(1, h2, h3, hs1); step(2, h4, h5, hs2); step(3, h6, h7, hs3);
    *(LAS f32x4*)(aim ^ (8 * K1)) = a[2];
    *(LAS f32x4*)((aim ^ (8 * K1)) + 1024) = a[3];
    bar();
    TS(9);
    f32x4 pa0, pa1;
    lr4(pa0, aim, 0); lr4(pa1, aim, 1024);
    const auto ra = __builtin_amdgcn_permlane16_swap(res.x, res.z, false, false);
    const auto rb = __builtin_amdgcn_permlane16_swap(res.y, res.w, false, false);
    lwait<0>(); tie(pa0); tie(pa1);
    a[0] += pa0; a[1] += pa1;
    x1v.x = pk(a[0].x + blo(ra[0]), a[0].y + bhi(ra[0]));
    x1v.y = pk(a[0].z + blo(rb[0]), a[0].w + bhi(rb[0]));
    x1v.z = pk(a[1].x + blo(ra[1]), a[1].y + bhi(ra[1]));
    x1v.w = pk(a[1].z + blo(rb[1]), a[1].w + bhi(rb[1]));
    x1v = nosent(x1v);
    st4_sc1(x1v, sws, l16, mine + (8 + w) * K1);
    const bf16x2 o2 = asb2(onesv.x);
    float s1 = 0.f, s2 = 0.f;
#pragma unroll
    for (int i = 0; i < 4; i++) {
      const bf16x2 v = asb2(x1v[i]);
      s1 = __builtin_amdgcn_fdot2_f32_bf16(v, o2, s1, false);
      s2 = __builtin_amdgcn_fdot2_f32_bf16(v, v, s2, false);
    }
    const auto e1 = __builtin_amdgcn_permlane32_swap(asu(s1), asu(s2), false, false);
    const unsigned e2 = asu(asf(e1[0]) + asf(e1[1]));
    const auto e3 = __builtin_amdgcn_permlane16_swap(e2, e2, false, false);
    const unsigned e4 = asu(asf(e3[0]) + asf(e3[1]));
    st1_sc1(umin(e4, 0xFFFFFFFEu), sws, (q & 1) ? OOB : (q >> 1) * 64 + t * 4, mine + 44 * K1 + w * 128);
  }
  TS(10);
  auto xdma = [&](bool first) {
#pragma unroll
    for (int i = 0; i < 8; i++) {
      if (first && (OPT & 2)) dma(sws, l16, hsrc + ((i >> 1) * 48 + 8 + (i & 1)) * K1, w * 8 * K1 + i * K1);
      else dma_sc1(sws, l16, hsrc + ((i >> 1) * 48 + 8 + (i & 1)) * K1, w * 8 * K1 + i * K1);
    }
  };
  auto xchk = [&]() {
#pragma nounroll
    for (unsigned s = 0; s < SPIN_CAP; s++) {
      v4u f[8];
#pragma unroll
      for (int i = 0; i < 8; i++) lr4(f[i], aim, i * 1024);
      lwait<0>();
      unsigned mx = 0;
#pragma unroll
      for (int i = 0; i < 8; i++) { tie(f[i]); mx = max4(mx, f[i]); }
      if (!anyset(mx)) break;
      xdma(false); vwait<0>();
      RT(2, 1);
    }
  };
  Fr w4[7][4];
  unsigned a4[4];
#pragma unroll
  for (int j = 0; j < 4; j++) a4[j] = opq(af[j] + (w - hf) * 8 * K1);
  auto w4rd = [&](int s, int pr) {
#pragma unroll
    for (int j = 0; j < 4; j++) frr(w4[s][j], pr == 0 ? opq(a4[j]) + (r1 - r2) : a4[j], pr == 2 ? 4096 : 0, 2048);
  };
  v4u gf[2][7];
  const unsigned gsrc = gbase + w * WGB + 16 * K1;
  auto gofs = [&](int c, int s) { unsigned ks = s + kr; ks = ks >= 7 ? ks - 7 : ks; return gsrc + (7 * c + ks) * K1; };
  auto gld = [&](int c, int s) { if (OPT & 4) ld4<0>(gf[c & 1][s], sws, l16, gofs(c, s)); else ld4_sc1<0>(gf[c & 1][s], sws, l16, gofs(c, s)); };

  if (w7) {
    vwait<2>();
    tie(touch);
    w4rd(0, 1); w4rd(1, 2);
    lwait<0>();
    w4dma(2, 1); w4dma(3, 2);
    asm volatile("s_barrier" ::: "memory");
    xdma(true);
    v4u sp[8];
#pragma unroll
    for (int i = 0; i < 8; i++) ld4_sc1<0>(sp[i], sws, l16, gbase + i * WGB + 44 * K1);
    asm volatile("s_barrier" ::: "memory");
    st2_sc1(tag, sws, (m == 0 && lane == 0) ? 0 : OOB, WS_EP + group * 128);
    vwait<9>();
    xchk();
    TS(11);
    bar();
    TS(12);
    vwait<1>();
#pragma unroll
    for (int i = 0; i < 28; i++) st4_sc1(SENT, sws, NOCLR ? OOB : l16, mine + (16 + i) * K1);
#pragma nounroll
    for (unsigned n = 0; n < SPIN_CAP; n++) {
      unsigned mx = 0;
#pragma unroll
      for (int i = 0; i < 8; i++) { tie(sp[i]); mx = max4(mx, sp[i]); }
      if (!anyset(mx)) break;
#pragma unroll
      for (int i = 0; i < 8; i++) rel4_sc1(sp[i], sws, l16, gbase + i * WGB + 44 * K1);
      vwait<0>();
      RT(2, 256);
    }
    {
      f32x4 v[8];
#pragma unroll
      for (int i = 0; i < 8; i++) v[i] = __builtin_bit_cast(f32x4, sp[i]);
      f32x4 u = ((v[0] + v[1]) + (v[2] + v[3])) + ((v[4] + v[5]) + (v[6] + v[7]));
#pragma unroll
      for (int i = 0; i < 4; i++) {
        const float s1 = u[i] + ror8(u[i]);
        const float s2 = __builtin_bit_cast(float, __builtin_amdgcn_update_dpp(0, __builtin_bit_cast(int, s1), 0x104, 0xf, 0xf, false));
        const f32x4 st = mkstats(s1, s2);
        if ((lane & 12) == 0) *(LAS f32x4*)(L_ST3 + (q * 16 + 4 * p4 + i) * 16) = st;
      }
    }
    TS(13);
    vwait<0>();
    lds_set(L_FLG, 1);
    st2_sc1(tag, sws, lane == 0 ? m * 8 : OOB, flA + 64);
    TS(14);
    w4rd(2, 1); w4rd(3, 2);
    lwait<0>();
    w4dma(4, 1); w4dma(5, 2);
    vwait<0>();
    w4rd(4, 1); w4rd(5, 2);
    lwait<0>();
    w4dma(6, 1);
    vwait<0>();
    w4rd(6, 1);
    TS(15);
    fa = 0;
    unsigned r = chkflag(fa, sws, (lane & 7) * 8, flA + 64, tag, tag, ~0ull);
    RT(0, r << 8); (void)r;
    TS(16);
    bar();
    TS(17);
#pragma unroll
    for (int s = 0; s < 14; s++) gld(s / 7, s % 7);
  } else {
    Fr w3[16][2];
    f32x4 cs0 = {0, 0, 0, 0}, cs1 = cs0;
    const unsigned a3 = (4 * q + r4) * 64 + ((q & 1) << 5) + 8 * p4;
    const unsigned a3l0 = opq(a3 + r1), a3l1 = opq(a3l0 ^ 32), a3h0 = opq(a3 + r2), a3h1 = opq(a3h0 ^ 32);
    auto w3rd = [&](int s) {
      const int sl = s % 6;
      frr(w3[s][0], sl < 2 ? a3l0 : a3h0, (sl < 2 ? sl : sl - 2) * 2048, 1024);
      frr(w3[s][1], sl < 2 ? a3l1 : a3h1, (sl < 2 ? sl : sl - 2) * 2048, 1024);
    };
    auto w3cs = [&](int s) { tie(w3[s][0]); tie(w3[s][1]); cs0 = mfma(fv(w3[s][0]), ones, cs0); cs1 = mfma(fv(w3[s][1]), ones, cs1); };
    vwait<2>();
#pragma unroll
    for (int s = 0; s < 6; s++) w3rd(s);
    lwait<12>();
    w3dma(6); w3dma(7); w3dma(8);
    asm volatile("s_barrier" ::: "memory");
    xdma(true);
    bar();
    w3dma(9); w3dma(10); w3dma(11);
#pragma unroll
    for (int s = 0; s < 6; s++) w3cs(s);
    tie(cs0); tie(cs1);
    vwait<14>();
    asm volatile("s_nop 7" ::: "memory");
    w3rd(6); w3rd(7); w3rd(8);
    lwait<0>();
    w3cs(6); w3cs(7); w3cs(8);
    vwait<6>();
    xchk();
    TS(11);
    bar();
    TS(12);
    if (w < 3) __builtin_amdgcn_s_setprio(2); else __builtin_amdgcn_s_setprio(1);
    f32x4 acc[4][2] = {};
    v4u xq[2][4];
    auto xrd = [&](int s) {
      const unsigned xs = opq(l16) + ((((s >> 1) + gi) & 7) << 11);
#pragma unroll
      for (int c = 0; c < 4; c++) lr4(xq[s & 1][c], xs, c * CELL + (s & 1) * 1024);
    };
    xrd(0); xrd(1);
#pragma unroll
    for (int s = 0; s < 16; s++) {
      const int ex = (s == 4 || s == 7) ? 12 : s == 10 ? 4 : s == 13 ? 8 : 0;
      lwaitn(s == 15 ? 0 : ex == 12 ? 15 : 4 + ex);
#pragma unroll
      for (int c = 0; c < 4; c++) tie(xq[s & 1][c]);
      SB();
#pragma unroll
      for (int c = 0; c < 4; c++) {
        acc[c][0] = mfma(fv(w3[s][0]), asb(xq[s & 1][c]), acc[c][0]);
        acc[c][1] = mfma(fv(w3[s][1]), asb(xq[s & 1][c]), acc[c][1]);
        SB();
        if (c == 1) {
          if (s < 3) w3dma(12 + s);
          if (s == 5) w3dma(15);
          if (s == 6) w4dma(0, 2);
          if (s == 8) w4dma(1, 0);
          if (s == 11) w4dma(2, 1);
          if (s == 14) w4dma(3, 2);
        }
      }
      if (s >= 5 && s < 8) w3cs(s + 4);
      if (s >= 8 && s < 12) w3cs(s + 4);
      SB();
      if (s < 14) xrd(s + 2);
      if (s == 3) { vwait<6>(); w3rd(9); w3rd(10); w3rd(11); }
      if (s == 6) { vwait<6>(); w3rd(12); w3rd(13); w3rd(14); }
      if (s == 9) { vwait<8>(); w3rd(15); }
      if (s == 12) { vwait<8>(); w4rd(0, 2); }
    }
    TS(13);
    if (w < 3) __builtin_amdgcn_s_setprio(0);
    vwait<8>();
    w4rd(1, 0);
    lds_wait_gt(L_FLG, 0);
    f32x4 st[4];
#pragma unroll
    for (int c = 0; c < 4; c++) lr4(st[c], L_ST3 + t * 16, c * 256);
#pragma unroll
    for (int c = 0; c < 4; c++) {
      if (c == 0) w4dma(4, 0);
      lwaitn(c == 0 ? 3 : 0);
      if (c == 1) w4dma(5, 1);
      if (c == 2) w4dma(6, 2);
      tie(st[c]);
      st4_sc1(nosent(gelu_frag(acc[c][0], acc[c][1], cs0, cs1, st[c])), sws, l16, mine + (16 + 7 * c + w) * K1);
      if (c == 0) { vwait<9>(); w4rd(2, 1); }
      if (c == 1) { vwait<10>(); w4rd(3, 2); }
      if (c == 0) TS(14);
      if (c == 3) TS(16);
    }
    __builtin_amdgcn_s_setprio(0);
    bar();
    TS(17);
#pragma unroll
    for (int s = 0; s < 7; s++) gld(0, s);
    vwait<9>();
    w4rd(4, 0); w4rd(5, 1); w4rd(6, 2);
    TS(15);
#pragma unroll
    for (int s = 0; s < 7; s++) gld(1, s);
    lwait<0>();
  }
#pragma unroll
  for (int s = 0; s < 7; s++)
#pragma unroll
    for (int j = 0; j < 4; j++) tie(w4[s][j]);
  {
    const unsigned apw = r2 + t * 256 + ((8 * hf) ^ q ^ t) * 16;
    unsigned pw[4];
#pragma unroll
    for (int j = 0; j < 4; j++) pw[j] = opq(apw ^ (64 * j));
    const unsigned rr = 2 * w + (lane >> 5);
    const unsigned apr = opq(L_W2 + rr * 256 + ((((lane & 31) >> 1) ^ rr) << 4) + 8 * (lane & 1));
    const unsigned vo = (row0 + rr) * 1024 + 128 * m + 4 * (lane & 31);
    f32x2 pr[8];
    auto fin = [&](int c, int n) {
      lwaitn(n);
#pragma unroll
      for (int p = 0; p < 8; p++) tie(pr[p]);
      f32x2 o = ((pr[0] + pr[1]) + (pr[2] + pr[3])) + ((pr[4] + pr[5]) + (pr[6] + pr[7]));
      st1(pk(o.x, o.y), so, vo, c * CELL);
    };
#pragma unroll
    for (int c = 0; c < 4; c++) {
      const unsigned mk = rq == c ? ~0u : 0u;
      f32x4 a[4] = {};
      a[0] = (f32x4){blo(x1v.x & mk), bhi(x1v.x & mk), blo(x1v.y & mk), bhi(x1v.y & mk)};
      a[1] = (f32x4){blo(x1v.z & mk), bhi(x1v.z & mk), blo(x1v.w & mk), bhi(x1v.w & mk)};
      vwaitn(c < 3 ? 7 : 1);
#pragma nounroll
      for (unsigned n = 0; n < SPIN_CAP; n++) {
        unsigned mx = 0;
#pragma unroll
        for (int s = 0; s < 7; s++) { tie(gf[c & 1][s]); mx = max4(mx, gf[c & 1][s]); }
        if (!anyset(mx)) break;
#pragma unroll
        for (int s = 0; s < 7; s++) rel4_sc1(gf[c & 1][s], sws, l16, gofs(c, s));
        vwait<0>();
        RT(3, 1u << (8 * c));
      }
      if (c == 0) TS(18);
      if (c == 2) TS(23);
      if (c == 3) TS(26);
#pragma unroll
      for (int s = 0; s < 7; s++) {
        SB();
#pragma unroll
        for (int j = 0; j < 4; j++) a[j] = mfma(fv(w4[s][j]), asb(gf[c & 1][s]), a[j]);
        SB();
        if (c < 2) gld(c + 2, s);
        if (c >= 2 && s == 3) fin(c - 2, 0);
      }
      if (c == 2) TS(24);
      if (c > 0) {
        bar();
        if (c == 2) TS(25);
#pragma unroll
        for (int p = 0; p < 8; p++) lr2(pr[p], apr, ((c - 1) & 1) * 4096 + p * 8 * K1);
      }
#pragma unroll
      for (int j = 0; j < 4; j++) lw4(pw[j], a[j], (c & 1) * 4096);
      if (c == 0) TS(19);
      if (c == 1) TS(20);
      if (c == 3) TS(21);
    }
    fin(2, 4);
    bar();
#pragma unroll
    for (int p = 0; p < 8; p++) lr2(pr[p], apr, 4096 + p * 8 * K1);
    fin(3, 0);
  }
}

extern "C" void kernel_launch(void* const* d_in, const int* in_sizes, int n_in, void* d_out, int out_size, void* d_ws, size_t ws_size,
                              hipStream_t stream) {
  (void)hipFuncSetAttribute((const void*)mixer, hipFuncAttributeMaxDynamicSharedMemorySize, L_TOTAL);
  mixer<<<256, 512, L_TOTAL, stream>>>(d_in[0], d_in[1], d_in[2], d_in[3], d_in[4], d_out, d_ws);
}
```

```cpp
#include <hip/hip_runtime.h>
#define LAS __attribute__((address_space(3)))
typedef __bf16 bf16x8 __attribute__((ext_vector_type(8)));
typedef __bf16 bf16x2 __attribute__((ext_vector_type(2)));
typedef short s16x4 __attribute__((ext_vector_type(4)));
typedef short s16x8 __attribute__((ext_vector_type(8)));
typedef float f32x4 __attribute__((ext_vector_type(4)));
typedef float f32x2 __attribute__((ext_vector_type(2)));
typedef unsigned v4u __attribute__((ext_vector_type(4)));
typedef unsigned v2u __attribute__((ext_vector_type(2)));
typedef __amdgpu_buffer_rsrc_t srd_t;
typedef unsigned long long u64;

constexpr unsigned K1 = 1024u, CELL = 16 * K1;
constexpr unsigned WS_EP = 0, WS_FL = 8192, WS_D = 65536, WGB = 48 * K1, WS_END = WS_D + 256u * WGB;
constexpr unsigned L_W2 = 64 * K1, L_R1 = 128 * K1, L_ST1 = 158 * K1, L_CS = 157 * K1 + 512, L_ST3 = 156 * K1, L_FLG = 157 * K1, L_TOTAL = 160 * K1;
#define NEWLAY 1
#define PH 2
#define PG 0
#define OSC 1
#define GI 6
#define GO 0
#define CPF 0
#define GC 2
#define LD3 1
#define LD2 1
#define XB 2
#define CS8 1
#define XSL 0
#define XD 2
#define E3 7
#define IL 1
#define GB 1
#define FAKE3 0
#define ROT 0
#define OPT 0
#define CP 0
#define GP 2
#define HL0 15
#define GO7 7
constexpr unsigned OOB = 0x80000000u;
#define SPINC 200000u
constexpr unsigned SPIN_CAP = SPINC;

#define FI __device__ __forceinline__
#define SB() __builtin_amdgcn_sched_barrier(0)
FI void dma(srd_t s, unsigned voff, unsigned soff, unsigned lds) {
  asm volatile("s_mov_b32 m0, %0\n\ts_nop 0\n\tbuffer_load_dwordx4 %1, %2, %3 offen lds" ::"s"(lds), "v"(voff), "s"(s), "s"(soff) : "memory");
}
FI void dma_sc1(srd_t s, unsigned voff, unsigned soff, unsigned lds) {
  asm volatile("s_mov_b32 m0, %0\n\ts_nop 0\n\tbuffer_load_dwordx4 %1, %2, %3 offen sc1 lds" ::"s"(lds), "v"(voff), "s"(s), "s"(soff) : "memory");
}
template <int OFF> FI void ld4_sc1(v4u& d, srd_t s, unsigned voff, unsigned soff) {
  asm volatile("buffer_load_dwordx4 %0, %1, %2, %3 offen offset:%4 sc1" : "=v"(d) : "v"(voff), "s"(s), "s"(soff), "n"(OFF) : "memory");
}
FI void reld2_sc1(v4u& d0, v4u& d1, srd_t s, unsigned voff, unsigned soff) {
  asm volatile("buffer_load_dwordx4 %0, %2, %3, %4 offen sc1\n\tbuffer_load_dwordx4 %1, %2, %3, %4 offen offset:1024 sc1\n\ts_waitcnt vmcnt(0)"
               : "+v"(d0), "+v"(d1) : "v"(voff), "s"(s), "s"(soff) : "memory");
}
template <int OFF> FI void ld4(v4u& d, srd_t s, unsigned voff, unsigned soff) {
  asm volatile("buffer_load_dwordx4 %0, %1, %2, %3 offen offset:%4" : "=v"(d) : "v"(voff), "s"(s), "s"(soff), "n"(OFF) : "memory");
}
template <int OFF, int BIT> FI void ld4o(v4u& d, srd_t s, unsigned voff, unsigned soff) { if (OPT & BIT) ld4<OFF>(d, s, voff, soff); else ld4_sc1<OFF>(d, s, voff, soff); }
FI void ld2_sc1(u64& d, srd_t s, unsigned voff, unsigned soff) {
  asm volatile("buffer_load_dwordx2 %0, %1, %2, %3 offen sc1" : "=v"(d) : "v"(voff), "s"(s), "s"(soff) : "memory");
}
FI void st4_sc1(v4u d, srd_t s, unsigned voff, unsigned soff) {
  asm volatile("buffer_store_dwordx4 %0, %1, %2, %3 offen sc1\n\ts_nop 1" ::"v"(d), "v"(voff), "s"(s), "s"(soff) : "memory");
}
FI void st2(v2u d, srd_t s, unsigned voff, unsigned soff) {
  if (OSC == 1) asm volatile("buffer_store_dwordx2 %0, %1, %2, %3 offen sc1" ::"v"(d), "v"(voff), "s"(s), "s"(soff) : "memory");
  else if (OSC == 2) asm volatile("buffer_store_dwordx2 %0, %1, %2, %3 offen sc0 sc1 nt" ::"v"(d), "v"(voff), "s"(s), "s"(soff) : "memory");
  else asm volatile("buffer_store_dwordx2 %0, %1, %2, %3 offen" ::"v"(d), "v"(voff), "s"(s), "s"(soff) : "memory");
}
FI void rel1_sc1(v4u& d, srd_t s, unsigned voff, unsigned soff) {
  asm volatile("buffer_load_dwordx4 %0, %1, %2, %3 offen sc1\n\ts_waitcnt vmcnt(0)" : "+v"(d) : "v"(voff), "s"(s), "s"(soff) : "memory");
}
FI void sbar() { asm volatile("s_barrier" ::: "memory"); }
FI void lw4u(unsigned a, v4u d, int off) { asm volatile("ds_write_b128 %0, %1 offset:%2" ::"v"(a), "v"(d), "i"(off) : "memory"); }
FI void st1(unsigned d, srd_t s, unsigned voff, unsigned soff) {
  asm volatile("buffer_store_dword %0, %1, %2, %3 offen" ::"v"(d), "v"(voff), "s"(s), "s"(soff) : "memory");
}
FI void st1_sc1(unsigned d, srd_t s, unsigned voff, unsigned soff) {
  asm volatile("buffer_store_dword %0, %1, %2, %3 offen sc1" ::"v"(d), "v"(voff), "s"(s), "s"(soff) : "memory");
}
FI void lw4(unsigned a, f32x4 d, int off) { asm volatile("ds_write_b128 %0, %1 offset:%2" ::"v"(a), "v"(d), "i"(off) : "memory"); }
FI void st2_sc1(u64 d, srd_t s, unsigned voff, unsigned soff) {
  asm volatile("buffer_store_dwordx2 %0, %1, %2, %3 offen sc1" ::"v"(d), "v"(voff), "s"(s), "s"(soff) : "memory");
}
template <int N> FI void vwait() { asm volatile("s_waitcnt vmcnt(%0)" ::"n"(N) : "memory"); }
template <int N> FI void lwait() { asm volatile("s_waitcnt lgkmcnt(%0)" ::"n"(N) : "memory"); }
FI void bar() { asm volatile("s_waitcnt lgkmcnt(0)\n\ts_barrier" ::: "memory"); }
template <class T> FI void tie(T& a) { asm volatile("" : "+v"(a)); }
struct Fr { s16x4 lo, hi; };
FI void tie(Fr& f) { asm volatile("" : "+v"(f.lo), "+v"(f.hi)); }
FI void trr(s16x4& d, unsigned a, int off) { asm volatile("ds_read_b64_tr_b16 %0, %1 offset:%2" : "=v"(d) : "v"(a), "i"(off) : "memory"); }
FI void frr(Fr& f, unsigned a, int off, int sec) { trr(f.lo, a, off); trr(f.hi, a, off + sec); }
template <class T> FI void lr4(T& d, unsigned a, int off) { asm volatile("ds_read_b128 %0, %1 offset:%2" : "=v"(d) : "v"(a), "i"(off) : "memory"); }
template <class T> FI void lr2(T& d, unsigned a, int off) { asm volatile("ds_read_b64 %0, %1 offset:%2" : "=v"(d) : "v"(a), "i"(off) : "memory"); }
FI void vwaitn(int n) { asm volatile("s_waitcnt vmcnt(%0)" ::"i"(n) : "memory"); }
FI void lwaitn(int n) { asm volatile("s_waitcnt lgkmcnt(%0)" ::"i"(n) : "memory"); }
FI bf16x8 fv(const Fr& f) { return __builtin_bit_cast(bf16x8, __builtin_shufflevector(f.lo, f.hi, 0, 1, 2, 3, 4, 5, 6, 7)); }
FI f32x4 mfma(bf16x8 a, bf16x8 b, f32x4 c) { return __builtin_amdgcn_mfma_f32_16x16x32_bf16(a, b, c, 0, 0, 0); }
FI bf16x8 asb(v4u v) { return __builtin_bit_cast(bf16x8, v); }
FI unsigned pk(float a, float b) {
  f32x2 f = {a, b};
  return __builtin_bit_cast(unsigned, __builtin_convertvector(f, bf16x2));
}
FI float asf(unsigned u) { return __builtin_bit_cast(float, u); }
FI unsigned asu(float u) { return __builtin_bit_cast(unsigned, u); }
FI bf16x2 asb2(unsigned u) { return __builtin_bit_cast(bf16x2, u); }
FI unsigned umin(unsigned a, unsigned b) { return a < b ? a : b; }
FI unsigned umax(unsigned a, unsigned b) { return a > b ? a : b; }
FI unsigned max4(unsigned m, v4u v) { return umax(umax(umax(m, v.x), v.y), umax(v.z, v.w)); }
FI bool anyset(unsigned mx) { return __builtin_amdgcn_ballot_w64(mx == ~0u) != 0; }
FI float blo(unsigned u) { return __builtin_bit_cast(float, u << 16); }
FI float bhi(unsigned u) { return __builtin_bit_cast(float, u & 0xffff0000u); }
FI unsigned opq(unsigned a) { asm volatile("" : "+v"(a)); return a; }
FI float gelu_n(float a, float k1, float k2, float irs) {
  float u = __builtin_fmaf(a * a, k2, k1);
  float e = __builtin_amdgcn_exp2f(a * u);
  return a * __builtin_amdgcn_rcpf(__builtin_fmaf(e, irs, irs));
}
FI v4u gelu_frag(f32x4 a0, f32x4 a1, f32x4 c0, f32x4 c1, f32x4 st) {
  float o[8];
#pragma unroll
  for (int j = 0; j < 4; j++) {
    o[j] = gelu_n(__builtin_fmaf(-st.x, c0[j], a0[j]), st.y, st.z, st.w);
    o[4 + j] = gelu_n(__builtin_fmaf(-st.x, c1[j], a1[j]), st.y, st.z, st.w);
  }
  v4u r = {pk(o[0], o[1]), pk(o[2], o[3]), pk(o[4], o[5]), pk(o[6], o[7])};
  return r;
}
FI f32x4 mkstats(float s1, float s2) {
  float mean = s1 * (1.f / 512.f);
  float var = __builtin_fmaf(s2, 1.f / 512.f, -mean * mean);
  var = (var > 0.f ? var : 0.f) + 1e-5f;
  float rstd = __builtin_amdgcn_rsqf(var);
  f32x4 r = {mean, -2.302208198f * rstd, -0.1029432397f * rstd * rstd * rstd, var * rstd};
  return r;
}
FI float diag(f32x4 sq, unsigned t) { return (t & 2) ? ((t & 1) ? sq.w : sq.z) : ((t & 1) ? sq.y : sq.x); }
FI void lds_wait_gt(unsigned addr, unsigned v) {
  unsigned x, n;
  asm volatile("s_mov_b32 %1, %4\n1:\n\tds_read_b32 %0, %2\n\ts_waitcnt lgkmcnt(0)\n\tv_cmp_lt_u32 vcc, %3, %0\n\ts_cbranch_vccnz 2f\n\t"
               "s_sub_u32 %1, %1, 1\n\ts_cbranch_scc0 1b\n2:"
               : "=&v"(x), "=&s"(n) : "v"(addr), "i"(v), "i"(SPIN_CAP * 16u) : "memory", "vcc", "scc");
}
FI unsigned chkflag(u64& f, srd_t s, unsigned voff, unsigned soff, u64 lo, u64 hi, u64 mask) {
  unsigned n; u64 tmp;
  asm volatile("s_mov_b32 %1, %9\n1:\n\tv_cmp_gt_u64 vcc, %6, %0\n\tv_cmp_lt_u64 %2, %7, %0\n\ts_or_b64 vcc, vcc, %2\n\t"
               "s_and_b64 vcc, vcc, %8\n\ts_cbranch_scc0 2f\n\tbuffer_load_dwordx2 %0, %3, %4, %5 offen sc1\n\ts_waitcnt vmcnt(0)\n\t"
               "s_sub_u32 %1, %1, 1\n\ts_cbranch_scc0 1b\n2:"
               : "+v"(f), "=&s"(n), "=&s"(tmp) : "v"(voff), "s"(s), "s"(soff), "s"(lo), "s"(hi), "s"(mask), "i"(SPIN_CAP) : "memory", "vcc", "scc");
  return SPIN_CAP - n;
}
FI void rel4_sc1(v4u& d, srd_t s, unsigned voff, unsigned soff) {
  asm volatile("buffer_load_dwordx4 %0, %1, %2, %3 offen sc1" : "+v"(d) : "v"(voff), "s"(s), "s"(soff) : "memory");
}
FI v4u nosent(v4u v) { v4u r = {umin(v.x, 0xFFFFFFFEu), umin(v.y, 0xFFFFFFFEu), umin(v.z, 0xFFFFFFFEu), umin(v.w, 0xFFFFFFFEu)}; return r; }
FI float ror8(float v) { return __builtin_bit_cast(float, __builtin_amdgcn_mov_dpp(__builtin_bit_cast(int, v), 0x128, 0xf, 0xf, true)); }
FI void lds_set(unsigned addr, unsigned v) { asm volatile("ds_write_b32 %0, %1" ::"v"(addr), "v"(v) : "memory"); }
#define TS(i)
#define RT(i, n)
FI void vwait2(bool w7, int a, int b) {
  if (w7) vwaitn(b); else vwaitn(a);
}
__global__ __launch_bounds__(512, 2) void mixer(const void* __restrict__ px, const void* __restrict__ pw1, const void* __restrict__ pw2,
                                                const void* __restrict__ pw3, const void* __restrict__ pw4, void* pout, void* pws) {
  const unsigned lane = threadIdx.x & 63;
  const unsigned w = __builtin_amdgcn_readfirstlane(threadIdx.x >> 6);
  const bool w7 = w == 7;
    const unsigned b = blockIdx.x, m = 4 * (b & 1) + ((b >> 3) & 3), gi = b >> 5, group = ((b & 7) >> 1) * 8 + gi, row0 = group * 64;
  const unsigned rq = w >> 1, hf = w & 1;
  const unsigned t = lane & 15, q = lane >> 4, r4 = (lane >> 2) & 3, p4 = lane & 3;
  const srd_t sx = __builtin_amdgcn_make_buffer_rsrc((void*)px, 0, 2048 * 1024, 0x00020000);
  const srd_t sw1 = __builtin_amdgcn_make_buffer_rsrc((void*)pw1, 0, 512 * 1024, 0x00020000);
  const srd_t sw2 = __builtin_amdgcn_make_buffer_rsrc((void*)pw2, 0, 512 * 1024, 0x00020000);
  const srd_t sw3 = __builtin_amdgcn_make_buffer_rsrc((void*)pw3, 0, 512 * 3584, 0x00020000);
  const srd_t sw4 = __builtin_amdgcn_make_buffer_rsrc((void*)pw4, 0, 1792 * 1024, 0x00020000);
  const srd_t so = __builtin_amdgcn_make_buffer_rsrc(pout, 0, 2048 * 1024, 0x00020000);
  const srd_t sws = __builtin_amdgcn_make_buffer_rsrc(pws, 0, WS_END, 0x00020000);
  asm volatile("" ::"s"(pw4), "s"(pout));
  u64 epoch;
  asm volatile("s_load_dwordx2 %0, %1, 0" : "=s"(epoch) : "s"((const char*)pws + WS_EP + group * 128) : "memory");
  unsigned pf = 0;
  if (CPF) {
    u64 pc;
    asm volatile("s_getpc_b64 %0" : "=s"(pc));
    unsigned o = ((b >> 3) * 8 + w) * 144;
    o = o < CPF - 192 ? o : CPF - 192;
    asm volatile("s_load_dword %0, %1, 0\n\ts_load_dword %0, %1, 64\n\ts_load_dword %0, %1, 128" : "+s"(pf) : "s"((pc & ~63ull) + (o & ~3u)) : "memory");
  }
  asm volatile("s_load_dword %0, %1, 0\n\ts_load_dword %0, %1, 64\n\ts_load_dword %0, %1, 128\n\ts_load_dword %0, %1, 192\n\t"
               "s_load_dword %0, %1, 256\n\ts_load_dword %0, %1, 320\n\ts_load_dword %0, %1, 384\n\ts_load_dword %0, %1, 448\n\t"
               "s_load_dword %0, %1, 512\n\ts_load_dword %0, %1, 576\n\ts_load_dword %0, %1, 640\n\ts_load_dword %0, %1, 704\n\t"
               "s_load_dword %0, %1, 768\n\ts_load_dword %0, %1, 832"
               : "+s"(pf) : "s"((const char*)pw3 + (b * 8 + w) * 896) : "memory");
  const unsigned l16 = lane * 16;
  const unsigned gbase = WS_D + group * 8 * WGB, mine = gbase + m * WGB;

  const unsigned rho = lane >> 3;
  const unsigned wsw = 128 * m + (((((lane & 7) >> 1) ^ q) << 1) | (lane & 1)) * 16;
  const unsigned vw2 = (8 * w + rho) * 1024 + wsw;
  const unsigned vw1 = (32 * (w >> 2) + 16 * (w & 1) + 4 * ((w >> 1) & 1) + 8 * (rho >> 2) + (rho & 3)) * 1024 + wsw;
  unsigned vxa, vxb;
  {
    const int cg = m >> 1;
    const int dl = (cg & 1) ? (cg >> 1) + 1 : -((cg >> 1) + 1);
    const int ra = (int)(row0 + 16 * rq + (t & 7)) + dl, rb = ra + 8;
    const unsigned cb = hf * 512 + 16 * q + 8 * (t & 8);
    vxa = (ra >= 0 && ra < 2048) ? ra * 1024 + cb : OOB;
    vxb = (rb >= 0 && rb < 2048) ? rb * 1024 + (cb ^ 64) : OOB;
  }
  v4u xa[4], xb[4], res;
  auto p1ld = [&](int p) {
    const unsigned pp = (p + gi + m) & 3;
    dma(sw1, vw1, pp * 64 * K1, p * CELL + w * K1);
    ld4<0>(xa[p], sx, vxa, pp * 128);
    dma(sw1, vw1, pp * 64 * K1 + 256 * K1, p * CELL + 8 * K1 + w * K1);
    ld4<0>(xb[p], sx, vxb, pp * 128);
  };
  const v4u SENT = {~0u, ~0u, ~0u, ~0u};
  if (CP == 0) st4_sc1(SENT, sws, lane < 8 ? l16 : OOB, mine + 44 * K1 + w * 128);
  p1ld(0);
  p1ld(1);
  sbar();
  if (CP == 0) {
    st4_sc1(SENT, sws, l16, mine + w * K1);
    st4_sc1(SENT, sws, l16, mine + (8 + w) * K1);
  }
  p1ld(2);
  p1ld(3);
  auto w2dma = [&](int p, int h2) { dma(sw2, vw2, (((p + gi) & 3) * 64 + h2 * 256) * K1, L_W2 + p * CELL + h2 * 8 * K1 + w * K1); };
  const unsigned r1 = L_R1 + w * 4 * K1, r2 = L_W2 + w * 8 * K1, re = w * 8 * K1 + 4 * K1;
  const unsigned vw3 = (lane >> 2) * 3584 + 448 * m + 64 * w + ((p4 ^ ((q & 1) << 1)) << 4);
  auto w3dma = [&](int s) {
    const unsigned so3 = ((s + 2 * gi) & 15) * 32 * 3584, sl = s & 7,
                   ld = sl < 2 ? r1 + sl * 2 * K1 : sl < 4 ? re + (sl - 2) * 2 * K1 : r2 + (sl - 4) * 2 * K1;
    dma(sw3, vw3, so3, ld);
    dma(sw3, vw3, so3 + 16 * 3584, ld + K1);
  };
  const unsigned kr = gi == 7 ? 0 : gi;
  auto w4dma = [&](int s, int pr) {
    unsigned k0 = kr, w0 = w;
    asm volatile("" : "+s"(k0), "+s"(w0));
    unsigned ks = s + k0; ks = ks >= 7 ? ks - 7 : ks;
#pragma unroll
    for (int i = 0; i < 4; i++)
      dma(sw4, vw2, (216 * w0 + 32 * ks + 8 * i) * K1, (pr == 0 ? L_R1 + w0 * 4 * K1 : L_W2 + w0 * 8 * K1 + (pr - 1) * 4 * K1) + i * K1);
  };
  auto gclr = [&](int i) { st4_sc1(SENT, sws, l16, mine + (16 + i + 4 * w) * K1); };
  u64 tag = 0;
  const unsigned flA = WS_FL + group * 128;
  const unsigned g2 = ((q & 1) << 1) | (r4 >> 1);
  unsigned af[4];
#pragma unroll
  for (int j = 0; j < 4; j++) af[j] = opq((4 * q + r4) * 128 + 8 * p4 + (((j ^ (2 * hf)) ^ g2) << 5) + hf * 8 * K1);
  TS(1);

  v4u onesv = {0x3F803F80u, 0x3F803F80u, 0x3F803F80u, 0x3F803F80u};
  tie(onesv);
  const bf16x8 ones = asb(onesv);
  const unsigned afc = opq((4 * q + r4) * 128 + 8 * p4 + ((rq ^ g2) << 5) + hf * 8 * K1);
  const unsigned l8 = (lane & 7) * 8;
  u64 fa;
  unsigned touch = 0;
  {
    f32x4 a[4] = {}, ap[4] = {}, rs0 = {0, 0, 0, 0}, sq0 = rs0, rs1 = rs0, sq1 = rs0, cc = rs0;
#pragma unroll
    for (int hp = 0; hp < 2; hp++) {
      if (hp == 0) vwait<10>(); else vwait<5>();
      bar();
      if (hp == 0) {
        asm volatile("" : "+s"(pf), "+s"(epoch));
        tag = epoch * 6364136223846793005ull + 1442695040888963407ull;
        TS(2);
      } else TS(3);
      Fr f[16], fc[4];
#pragma unroll
      for (int i = 0; i < 16; i++) {
        frr(f[i], af[i & 3], (2 * hp + (i >> 3)) * CELL + ((i >> 2) & 1) * 4096, 2048);
        if ((i & 3) == 3) frr(fc[i >> 2], afc, (2 * hp + (i >> 3)) * CELL + ((i >> 2) & 1) * 4096, 2048);
      }
      if (hp == 0) {
        if (CP == 1) {
          st4_sc1(SENT, sws, l16, mine + w * K1);
          st4_sc1(SENT, sws, l16, mine + (8 + w) * K1);
        }
      } else {
        if (w7 && CP == 0) st2_sc1(tag, sws, lane == 0 ? m * 8 : OOB, flA);
        w2dma(2, 0);
      }
      v4u x0[2], x1[2];
#pragma unroll
      for (int p = 0; p < 2; p++) {
        tie(xa[2 * hp + p]); tie(xb[2 * hp + p]);
        const bool lo = t < 8;
        x0[p] = lo ? xa[2 * hp + p] : xb[2 * hp + p];
        x1[p] = lo ? xb[2 * hp + p] : xa[2 * hp + p];
        rs0 = mfma(ones, asb(x0[p]), rs0); sq0 = mfma(asb(x0[p]), asb(x0[p]), sq0);
        rs1 = mfma(ones, asb(x1[p]), rs1); sq1 = mfma(asb(x1[p]), asb(x1[p]), sq1);
        tie(rs0); tie(sq0); tie(rs1); tie(sq1);
        SB();
        if (hp == 0 && p == 0) {
          if (CP == 1) st4_sc1(SENT, sws, lane < 8 ? l16 : OOB, mine + 44 * K1 + w * 128);
          ld4<0>(res, sx, (row0 + 16 * rq + t) * 1024 + 128 * m + 64 * hf + 16 * (((q & 1) << 1) | (q >> 1)), 0);
        } else if (hp == 0) w2dma(0, 0);
        else if (p == 0) w2dma(2, 1);
        else w2dma(3, 0);
      }
#pragma unroll
      for (int g = 0; g < 4; g++) {
        lwaitn(g == 0 ? 15 : g == 1 ? 15 : g == 2 ? 10 : 0);
#pragma unroll
        for (int j = 0; j < 4; j++) tie(f[4 * g + j]);
        tie(fc[g]);
        SB();
#pragma unroll
        for (int j = 0; j < 4; j++) {
          if (g & 1) ap[j] = mfma(fv(f[4 * g + j]), asb(x1[g >> 1]), ap[j]);
          else a[j] = mfma(fv(f[4 * g + j]), asb(x0[g >> 1]), a[j]);
        }
        cc = mfma(fv(fc[g]), ones, cc);
        SB();
        if (hp == 0) { if (g == 0) w2dma(0, 1); else if (g < 3) w2dma(1, g - 1); }
        else if (g == 0) w2dma(3, 1);
        else if (g >= 2 && !w7) w3dma(g - 2);
        else if (!w7 && GC >= 4) { gclr(0); gclr(1); if (GC == 4) { gclr(2); gclr(3); } }
      }
    }
#pragma unroll
    for (int j = 3; j >= 0; j--) {
      a[j].x += ror8(ap[j].x); a[j].y += ror8(ap[j].y); a[j].z += ror8(ap[j].z); a[j].w += ror8(ap[j].w);
      if (j >= 2) *(LAS f32x4*)(rq * 8 * K1 + hf * 2 * K1 + (j - 2) * K1 + l16) = a[j];
    }
    if (q == (t >> 2)) {
      f32x2 s0 = {rs0.x, diag(sq0, t)}, s1 = {rs1.x, diag(sq1, t)};
      *(LAS f32x2*)(L_ST1 + w * 256 + t * 8) = s0;
      *(LAS f32x2*)(L_ST1 + w * 256 + 128 + (t ^ 8) * 8) = s1;
    }
    if (t == 0) *(LAS f32x4*)(L_CS + hf * 256 + rq * 64 + q * 16) = cc;
    if (w7) { lds_set(L_FLG, 0); lds_set(L_FLG + 4, 0); lds_set(L_FLG + 8, 0); }
    vwait2(w7, GC == 4 ? 8 : GC == 5 ? 6 : 4, 0);
    bar();
    TS(4);
    f32x4 pa0, pa1, c00, c01, c10, c11;
    f32x2 s0, s1, s2, s3;
    {
      const unsigned as = L_ST1 + (w & 6) * 256 + t * 8, ac = L_CS + hf * 128 + q * 16, ae = rq * 8 * K1 + (hf ^ 1) * 2 * K1 + l16;
      lr2(s0, as, 0); lr2(s1, as, 128); lr2(s2, as, 256); lr2(s3, as, 384);
      lr4(pa0, ae, 0); lr4(pa1, ae, 1024);
      lr4(c00, ac, 0); lr4(c01, ac, 64); lr4(c10, ac, 256); lr4(c11, ac, 320);
    }
    ld2_sc1(fa, sws, l8, flA);
    lwait<6>(); tie(s0); tie(s1); tie(s2); tie(s3);
    f32x4 st = mkstats((s0.x + s1.x) + (s2.x + s3.x), (s0.y + s1.y) + (s2.y + s3.y));
    lwait<0>(); tie(pa0); tie(pa1); tie(c00); tie(c01); tie(c10); tie(c11);
    st4_sc1(nosent(gelu_frag(a[0] + pa0, a[1] + pa1, c00 + c10, c01 + c11, st)), sws, l16, mine + w * K1);
  }
  TS(5);
  v4u h0, h1, h2, h3, h4, h5, h6, h7;
  const unsigned hsrc = gbase + 4 * hf * WGB + 2 * rq * K1;
  const unsigned hs0 = hsrc + (gi & 3) * WGB, hs1 = hsrc + ((1 + gi) & 3) * WGB, hs2 = hsrc + ((2 + gi) & 3) * WGB, hs3 = hsrc + ((3 + gi) & 3) * WGB;
  v4u x1v;
  const unsigned aim = opq(w * 8 * K1 + l16);
  Fr w3[16][2];
  const unsigned a3 = (4 * q + r4) * 64 + ((q & 1) << 5) + 8 * p4;
  const unsigned a3l0 = opq(a3 + r1), a3l1 = opq(a3l0 ^ 32), a3h0 = opq(a3 + r2), a3h1 = opq(a3h0 ^ 32);
  const unsigned a3e0 = opq(a3 + re), a3e1 = opq(a3e0 ^ 32);
  auto w3rd = [&](int s) {
    const int sl = s & 7, o = (sl < 4 ? sl & 1 : sl - 4) * 2048;
    frr(w3[s][0], sl < 2 ? a3l0 : sl < 4 ? a3e0 : a3h0, o, 1024);
    frr(w3[s][1], sl < 2 ? a3l1 : sl < 4 ? a3e1 : a3h1, o, 1024);
  };
  auto chk2 = [&](v4u& d0, v4u& d1, unsigned src, int ri) {
    tie(d0); tie(d1);
    if (__builtin_expect(anyset(max4(max4(0, d0), d1)), 0)) {
#pragma nounroll
      for (unsigned n = 0; n < SPIN_CAP; n++) {
        reld2_sc1(d0, d1, sws, l16, src);
        RT(ri, 1);
        if (!anyset(max4(max4(0, d0), d1))) break;
      }
    }
  };
  {
    Fr u[32];
#pragma unroll
    for (int j = 0; j < 4; j++) af[j] = opq(af[j] + L_W2);
#pragma unroll
    for (int i = 0; i < 32; i++) {
      frr(u[i], af[i & 3], (i >> 3) * CELL + ((i >> 2) & 1) * 4096, 2048);
      if (i == GI) {
        sbar();
        if (!w7) {
          if (GO != 1) { w3dma(2); w3dma(3); }
          if (GO < 2) { gclr(0); gclr(1); }
          if (GO != 2) { gclr(2); gclr(3); }
          if (GO == 1) { w3dma(2); w3dma(3); }
        }
      }
      if (i == HL0) {
        vwait2(w7, GO == 2 ? 5 : GO == 3 ? 7 : 9, 1);
        unsigned r = chkflag(fa, sws, l8, flA, tag, tag, ~0ull);
        RT(0, r); (void)r;
        TS(6);
      }
      if (i == HL0) ld4o<0, 1>(h0, sws, l16, hs0);
      if (i == HL0 + 2) ld4o<1024, 1>(h1, sws, l16, hs0);
      if (i == HL0 + 4) ld4o<0, 1>(h2, sws, l16, hs1);
      if (i == HL0 + 6) ld4o<1024, 1>(h3, sws, l16, hs1);
      if (i == HL0 + 8) ld4o<0, 1>(h4, sws, l16, hs2);
      if (i == HL0 + 10) ld4o<1024, 1>(h5, sws, l16, hs2);
      if (i == HL0 + 12) ld4o<0, 1>(h6, sws, l16, hs3);
      if (i == HL0 + 14) ld4o<1024, 1>(h7, sws, l16, hs3);
    }
    if (GO >= 2 && !w7) { gclr(0); gclr(1); if (GO == 2) { gclr(2); gclr(3); } }
    bar();
    TS(7);
    auto w7a = [&]() {
      w4dma(0, 1); w4dma(1, 2);
      asm volatile("buffer_load_dword %0, %1, %2, 0 offen" : "+v"(touch) : "v"(lane < 56 ? (b * 56 + lane) * 128 : OOB), "s"(sw4) : "memory");
    };
    if (!LD2) { if (w7) w7a(); else { w3dma(4); w3dma(5); w3dma(6); w3dma(7); } }
    f32x4 a[4] = {};
    auto step = [&](int p, v4u& ha, v4u& hc, unsigned hs) {
      if (LD2) vwait2(w7, GO == 2 ? 10 : GO == 3 ? 8 : 6, p == 0 ? 6 : 15 - 2 * p); else vwait2(w7, 14 - 2 * p, 15 - 2 * p);
      chk2(ha, hc, hs, 1);
      if (LD2) { if (!w7) { w3dma(4 + p); if (GC == 1) gclr(p); } else if (p == 0) w7a(); }
      if (p == 0) { tie(res); TS(8); }
#pragma unroll
      for (int i = 0; i < 8; i++) tie(u[8 * p + i]);
      SB();
#pragma unroll
      for (int i = 0; i < 8; i++) a[(i + 2) & 3] = mfma(fv(u[8 * p + ((i + 2) & 3) + (i & 4)]), asb(i < 4 ? ha : hc), a[(i + 2) & 3]);
      SB();
    };
    step(0, h0, h1, hs0);
    asm volatile("s_nop 4" ::: "memory");
    w3rd(0); w3rd(1); w3rd(2); w3rd(3);
    step(1, h2, h3, hs1);
    step(2, h4, h5, hs2);
    step(3, h6, h7, hs3);
    *(LAS f32x4*)(aim ^ (8 * K1)) = a[2];
    *(LAS f32x4*)((aim ^ (8 * K1)) + 1024) = a[3];
    bar();
    TS(9);
    f32x4 pa0, pa1;
    lr4(pa0, aim, 0); lr4(pa1, aim, 1024);
    const auto ra = __builtin_amdgcn_permlane16_swap(res.x, res.z, false, false);
    const auto rb = __builtin_amdgcn_permlane16_swap(res.y, res.w, false, false);
    lwait<0>(); tie(pa0); tie(pa1);
    a[0] += pa0; a[1] += pa1;
    x1v.x = pk(a[0].x + blo(ra[0]), a[0].y + bhi(ra[0]));
    x1v.y = pk(a[0].z + blo(rb[0]), a[0].w + bhi(rb[0]));
    x1v.z = pk(a[1].x + blo(ra[1]), a[1].y + bhi(ra[1]));
    x1v.w = pk(a[1].z + blo(rb[1]), a[1].w + bhi(rb[1]));
    x1v = nosent(x1v);
    st4_sc1(x1v, sws, l16, mine + (8 + w) * K1);
    const bf16x2 o2 = asb2(onesv.x);
    float s1 = 0.f, s2 = 0.f;
#pragma unroll
    for (int i = 0; i < 4; i++) {
      const bf16x2 v = asb2(x1v[i]);
      s1 = __builtin_amdgcn_fdot2_f32_bf16(v, o2, s1, false);
      s2 = __builtin_amdgcn_fdot2_f32_bf16(v, v, s2, false);
    }
    const auto e1 = __builtin_amdgcn_permlane32_swap(asu(s1), asu(s2), false, false);
    const unsigned e2 = asu(asf(e1[0]) + asf(e1[1]));
    const auto e3 = __builtin_amdgcn_permlane16_swap(e2, e2, false, false);
    const unsigned e4 = asu(asf(e3[0]) + asf(e3[1]));
    st1_sc1(umin(e4, 0xFFFFFFFEu), sws, (q & 1) ? OOB : (q >> 1) * 64 + t * 4, mine + 44 * K1 + w * 128);
  }
  TS(10);
  auto xld = [&]() {
    ld4o<0, 2>(h0, sws, l16, hsrc + 8 * K1); ld4o<1024, 2>(h1, sws, l16, hsrc + 8 * K1);
    ld4o<0, 2>(h2, sws, l16, hsrc + 56 * K1); ld4o<1024, 2>(h3, sws, l16, hsrc + 56 * K1);
    ld4o<0, 2>(h4, sws, l16, hsrc + 104 * K1); ld4o<1024, 2>(h5, sws, l16, hsrc + 104 * K1);
    ld4o<0, 2>(h6, sws, l16, hsrc + 152 * K1); ld4o<1024, 2>(h7, sws, l16, hsrc + 152 * K1);
  };
  auto xput = [&](int i, v4u& d0, v4u& d1, int n) {
    vwaitn(n);
    chk2(d0, d1, hsrc + (48 * i + 8) * K1, 2);
    lw4u(aim, d0, i * 2048); lw4u(aim, d1, i * 2048 + 1024);
  };
  Fr w4[7][4];
  unsigned a4[4];
#pragma unroll
  for (int j = 0; j < 4; j++) a4[j] = opq(af[j] + (w - hf) * 8 * K1);
  auto w4rd = [&](int s, int pr) {
#pragma unroll
    for (int j = 0; j < 4; j++) frr(w4[s][j], pr == 0 ? opq(a4[j]) + (r1 - r2) : a4[j], pr == 2 ? 4096 : 0, 2048);
  };
  v4u gf[2][7];
  const unsigned crot = ROT ? m : 0;
  const unsigned gsrc = gbase + w * WGB + 16 * K1;
  auto gofs = [&](int c, int s) { unsigned ks = s + kr; ks = ks >= 7 ? ks - 7 : ks; return gsrc + (7 * ((c + crot) & 3) + ks) * K1; };
  auto gld = [&](int c, int s) { ld4o<0, 4>(gf[c & 1][s], sws, l16, gofs(c, s)); };

  if (w7) {
    sbar();
    vwait<2>();
    const unsigned vb = opq(vw3 - 448);
    auto help = [&](int v) {
#pragma unroll
      for (int s = 12; s < 16; s++) {
        const unsigned so3 = ((s + 2 * gi) & 15) * 32 * 3584 + 64 * v, ld = L_W2 + v * 8 * K1 + (s - 12) * 2 * K1;
        dma(sw3, vb, so3, ld);
        dma(sw3, vb, so3 + 16 * 3584, ld + K1);
      }
    };
    sbar();
    xld();
    v4u sp[8];
#pragma unroll
    for (int i = 0; i < 8; i++) ld4_sc1<0>(sp[i], sws, l16, gbase + i * WGB + 44 * K1);
    st2_sc1(tag, sws, (m == 0 && lane == 0) ? 0 : OOB, WS_EP + group * 128);
    st2_sc1(tag, sws, lane == 0 ? m * 8 : OOB, flA + 64);
    help(0); help(1); help(2);
    xput(0, h0, h1, 40); xput(1, h2, h3, 38); xput(2, h4, h5, 36); xput(3, h6, h7, 34);
    tie(touch);
    TS(11);
    bar();
    TS(12);
    w4rd(0, 1); w4rd(1, 2);
    vwait<0>();
    lds_set(L_FLG + 4, 1);
    help(3); help(4); help(5); help(6);
#pragma nounroll
    for (unsigned n = 0; n < SPIN_CAP; n++) {
      unsigned mx = 0;
#pragma unroll
      for (int i = 0; i < 8; i++) { tie(sp[i]); mx = max4(mx, sp[i]); }
      if (!anyset(mx)) break;
#pragma unroll
      for (int i = 0; i < 8; i++) rel4_sc1(sp[i], sws, l16, gbase + i * WGB + 44 * K1);
      vwait<0>();
      RT(2, 256);
    }
    {
      f32x4 v[8];
#pragma unroll
      for (int i = 0; i < 8; i++) v[i] = __builtin_bit_cast(f32x4, sp[i]);
      f32x4 u = ((v[0] + v[1]) + (v[2] + v[3])) + ((v[4] + v[5]) + (v[6] + v[7]));
#pragma unroll
      for (int i = 0; i < 4; i++) {
        const float s1 = u[i] + ror8(u[i]);
        const float s2 = __builtin_bit_cast(float, __builtin_amdgcn_update_dpp(0, __builtin_bit_cast(int, s1), 0x104, 0xf, 0xf, false));
        const f32x4 st = mkstats(s1, s2);
        if ((lane & 12) == 0) *(LAS f32x4*)(L_ST3 + (q * 16 + 4 * p4 + i) * 16) = st;
      }
    }
    lwait<0>();
    TS(13);
    lds_set(L_FLG, 1);
    w4dma(2, 1); w4dma(3, 2);
    vwait<8>();
    lds_set(L_FLG + 8, 1);
    TS(14);
    vwait<0>();
    w4rd(2, 1); w4rd(3, 2);
    lwait<0>();
    w4dma(4, 1); w4dma(5, 2);
    vwait<0>();
    w4rd(4, 1); w4rd(5, 2);
    lwait<0>();
    w4dma(6, 1);
    vwait<0>();
    w4rd(6, 1);
    TS(15);
    fa = 0;
    unsigned r = chkflag(fa, sws, l8, flA + 64, tag, tag, ~0ull);
    RT(0, r << 8); (void)r;
    TS(16);
    lds_wait_gt(L_FLG, GO7);
    TS(17);
#pragma unroll
    for (int s = 0; s < 14; s++) { if (s == 7 && GB) sbar(); gld(s / 7, s % 7); }
  } else {
    f32x4 cs0 = {0, 0, 0, 0}, cs1 = cs0;
    auto w3cs = [&](int s) { tie(w3[s][0]); tie(w3[s][1]); cs0 = mfma(fv(w3[s][0]), ones, cs0); cs1 = mfma(fv(w3[s][1]), ones, cs1); };
    sbar();
    w3dma(8); w3dma(9); w3dma(10); w3dma(11);
    vwait<10>();
    w3rd(4); w3rd(5); w3rd(6); w3rd(7);
    bar();
    TS(24);
    xld();
#pragma unroll
    for (int s = 0; s < 8; s++) w3cs(s);
    tie(cs0); tie(cs1);
    vwait<8>();
    TS(23);
    asm volatile("s_nop 7" ::: "memory");
    w3rd(8); w3rd(9); w3rd(10); w3rd(11);
    if (CS8) { lwait<0>(); w3cs(8); w3cs(9); w3cs(10); w3cs(11); tie(cs0); tie(cs1); }
    xput(0, h0, h1, 6); xput(1, h2, h3, 4); xput(2, h4, h5, 2); xput(3, h6, h7, 0);
    TS(11);
    if (w < 3) __builtin_amdgcn_s_setprio(PH); else __builtin_amdgcn_s_setprio(1);
    const unsigned fl3 = opq(w < 3 ? L_FLG + 4 : L_FLG + 8);
    unsigned xs;
    auto xad = [&](int s) {
      unsigned g0 = gi;
      asm volatile("" : "+s"(g0));
      asm volatile("v_add_u32 %0, %1, %2" : "=v"(xs) : "s"((((s >> 1) + g0) & 7) << 11), "v"(l16));
    };
    xad(0);
    bar();
    TS(12);
    f32x4 acc[4][2] = {};
    v4u xq[XD][4];
    auto xr1 = [&](int s, int c) { lr4(xq[s % XD][c], xs, c * CELL + (s & 1) * 1024); };
#pragma unroll
    for (int i = 0; i < 8; i++) xr1(i >> 2, i & 3);
    xad(2);
    SB();
    if (!CS8) { w3cs(8); w3cs(9); w3cs(10); w3cs(11); tie(cs0); tie(cs1); }
#pragma unroll
    for (int s = 0; s < 16; s++) {
      const int ex = s == E3 + 1 ? 16 : s == E3 + 3 ? 8 : 0;
      const bool ds = s == E3 + 2 || s == E3 + 4;
      const int nx = ds ? (s + XD <= 16 ? 4 : 0) : 4 * (XD - 1 < 15 - s ? XD - 1 : 15 - s);
      lwaitn(nx + ex < 15 ? nx + ex : 15);
#pragma unroll
      for (int c = 0; c < 4; c++) tie(xq[s % XD][c]);
      SB();
#pragma unroll
      for (int c = 0; c < 4; c++) {
        acc[c][0] = mfma(fv(w3[s][0]), asb(xq[s % XD][c]), acc[c][0]);
        acc[c][1] = mfma(fv(w3[s][1]), asb(xq[s % XD][c]), acc[c][1]);
        SB();
        if (IL && s < 14) xr1(s + 2, c);
        if (c == 1) {
          if (s == 0) w4dma(0, 0);
          if (s == E3 + 2) w4dma(1, 1);
          if (s == E3 + 4) w4dma(3, 0);
        }
        if (c == 3 && s == E3 + 2) w4dma(2, 2);
      }
      if (s == E3 + 2) { w3cs(12); w3cs(13); }
      if (s == E3 + 3) { w3cs(14); w3cs(15); }
      if (s == E3 + 2 || s == E3 + 3) { tie(cs0); tie(cs1); SB(); asm volatile("s_nop 7" ::: "memory"); }
      SB();
      if (!IL && s < 14) { xr1(s + 2, 0); xr1(s + 2, 1); xr1(s + 2, 2); xr1(s + 2, 3); }
      if ((s & 1) && s < 13) xad(s + 3);
      if (s == E3) { TS(25); lds_wait_gt(fl3, 0); TS(26); }
      if (s == E3) { w3rd(12); w3rd(13); w3rd(14); w3rd(15); }
      if (s == E3 + 2) { vwait<8>(); w4rd(0, 0); }
    }
    TS(13);
    if (w < 3) __builtin_amdgcn_s_setprio(PG);
    ld2_sc1(fa, sws, l8, flA + 64);
    lds_wait_gt(L_FLG, 0);
    f32x4 st[4];
#pragma unroll
    for (int c = 0; c < 4; c++) lr4(st[c], L_ST3 + t * 16, c * 256);
    lwait<0>();
#pragma unroll
    for (int c = 0; c < 4; c++) tie(st[c]);
#pragma unroll
    for (int c = 0; c < 4; c++) {
      st4_sc1(nosent(gelu_frag(acc[c][0], acc[c][1], cs0, cs1, st[c])), sws, l16, mine + (16 + 7 * c + w) * K1);
      if (c == 0) { vwait<10>(); w4rd(1, 1); }
      if (c == 1) { vwait<7>(); w4rd(2, 2); lwait<8>(); w4dma(4, 1); }
      if (c == 2) { vwait<7>(); w4rd(3, 0); lwait<8>(); w4dma(5, 2); }
      if (c == 3) { lwait<0>(); w4dma(6, 0); }
      if (c == GP && lane == 0) asm volatile("ds_add_u32 %0, %1" ::"v"(L_FLG), "v"(1u) : "memory");
      if (c == 0) TS(14);
      if (c == 3) TS(16);
    }
    __builtin_amdgcn_s_setprio(0);
    unsigned r = chkflag(fa, sws, l8, flA + 64, tag, tag, ~0ull);
    RT(0, r << 8); (void)r;
    lds_wait_gt(L_FLG, GO7);
    TS(17);
#pragma unroll
    for (int s = 0; s < 14; s++) { if (s == 7 && GB) sbar(); gld(s / 7, s % 7); }
    TS(15);
    vwait<14>();
    w4rd(4, 1); w4rd(5, 2); w4rd(6, 0);
  }
  lwait<0>();
#pragma unroll
  for (int s = 0; s < 7; s++)
#pragma unroll
    for (int j = 0; j < 4; j++) tie(w4[s][j]);
  {
    const unsigned apw = r2 + t * 256 + ((8 * hf) ^ q ^ t) * 16;
    unsigned pw[4];
#pragma unroll
    for (int j = 0; j < 4; j++) pw[j] = opq(apw ^ (64 * j));
#pragma unroll
    for (int c = 0; c < 4; c++) {
      const unsigned mk = rq == ((c + crot) & 3) ? ~0u : 0u;
      f32x4 a[4] = {};
      a[0] = (f32x4){blo(x1v.x & mk), bhi(x1v.x & mk), blo(x1v.y & mk), bhi(x1v.y & mk)};
      a[1] = (f32x4){blo(x1v.z & mk), bhi(x1v.z & mk), blo(x1v.w & mk), bhi(x1v.w & mk)};
      vwaitn(c < 3 ? 7 : 0);
      auto gmx = [&]() {
        unsigned mx = 0;
#pragma unroll
        for (int s = 0; s < 7; s++) { tie(gf[c & 1][s]); mx = max4(mx, gf[c & 1][s]); }
        return anyset(mx);
      };
      if (__builtin_expect(gmx(), 0)) {
#pragma nounroll
        for (unsigned n = 0; n < SPIN_CAP; n++) {
#pragma unroll
          for (int s = 0; s < 7; s++) rel4_sc1(gf[c & 1][s], sws, l16, gofs(c, s));
          vwait<0>();
          RT(3, 1u << (8 * c));
          if (!gmx()) break;
        }
      }
      if (c == 0) TS(18);
#pragma unroll
      for (int s = 0; s < 7; s++) {
        v4u& d = gf[c & 1][s];
        SB();
#pragma unroll
        for (int j = 0; j < 4; j++) a[j] = mfma(fv(w4[s][j]), asb(d), a[j]);
        SB();
        if (c < 2) gld(c + 2, s);
      }
      if (c == 2) {
#pragma unroll
        for (int j = 0; j < 4; j++) pw[j] = opq(pw[j] - L_W2);
      }
      asm volatile("s_nop 7" ::: "memory");
#pragma unroll
      for (int j = 0; j < 4; j++) lw4(pw[j], a[j], (c & 1) * 4096);
      if (c == 0) TS(19);
      if (c == 1) TS(20);
      if (c == 3) TS(21);
    }
    const unsigned rr = 8 * hf + q;
    const unsigned ar0 = opq((rq < 2 ? L_W2 : 0) + (rq & 1) * 4096 + rr * 256 + ((t ^ rr) << 4)), ar1 = opq((ar0 ^ 64) + 1024);
    const unsigned vo = (row0 + 16 * ((rq + crot) & 3) + rr) * 1024 + 128 * m + 8 * t;
    bar();
    f32x4 pr[16];
#pragma unroll
    for (int p = 0; p < 16; p++) lr4(pr[p], p < 8 ? ar0 : ar1, (p & 7) * 8 * K1);
#pragma unroll
    for (int e = 0; e < 2; e++) {
      lwaitn(e == 0 ? 8 : 0);
#pragma unroll
      for (int p = 0; p < 8; p++) tie(pr[8 * e + p]);
      const f32x4* r = pr + 8 * e;
      const f32x4 o = ((r[0] + r[1]) + (r[2] + r[3])) + ((r[4] + r[5]) + (r[6] + r[7]));
      const v2u ov = {pk(o.x, o.y), pk(o.z, o.w)};
      st2(ov, so, vo, e * 4 * K1);
    }
  }
}

extern "C" void kernel_launch(void* const* d_in, const int* in_sizes, int n_in, void* d_out, int out_size, void* d_ws, size_t ws_size,
                              hipStream_t stream) {
  (void)hipFuncSetAttribute((const void*)mixer, hipFuncAttributeMaxDynamicSharedMemorySize, L_TOTAL);
  mixer<<<256, 512, L_TOTAL, stream>>>(d_in[0], d_in[1], d_in[2], d_in[3], d_in[4], d_out, d_ws);
}
```
